```python
import torch

def _softmax(x, axis=-1):
    return torch.softmax(x, dim=axis)

def _gelu(x):
    return torch.nn.functional.gelu(x, approximate='tanh')

def _silu(x):
    return torch.nn.functional.silu(x)

def _sigmoid(x):
    return torch.sigmoid(x)

def _layernorm(x, axis=-1, eps=1e-05):
    m = x.mean(dim=axis, keepdim=True)
    v = x.var(dim=axis, keepdim=True, unbiased=False)
    return (x - m) / torch.sqrt(v + eps)

def _scatter_add(tgt, idx, src, axis=0):
    return tgt.clone().index_add_(axis, idx, src)

def _segment_sum(src, seg, n):
    out = torch.zeros((n,) + src.shape[1:], dtype=src.dtype)
    return out.index_add_(0, seg, src)

def _pad(x, pw):
    flat = tuple((v for pair in reversed(pw) for v in pair))
    return torch.nn.functional.pad(x, flat)
kernel_name = 'adaLN_cond__ssm_conv_s4__glu_gated_bilinear__prompt'

def setup_inputs():

    _w = lambda a: (lambda t: t.to(torch.bfloat16) if t.is_floating_point() else t)(torch.as_tensor(a))

    def _A_setup():
        B, L, D, N = (1, 2048, 448, 256)
        A_re = -(lambda _x: torch.exp(torch.as_tensor(_x)))((lambda *s: torch.randn(*s, dtype=torch.bfloat16) * 3.2)(D, N))
        A_im = torch.pi * (lambda *s: torch.randn(*s, dtype=torch.bfloat16) * 0.1)(D, N)
        B_re = (lambda *s: torch.randn(*s, dtype=torch.bfloat16) * 0.1)(D, N)
        B_im = (lambda *s: torch.randn(*s, dtype=torch.bfloat16) * 0.1)(D, N)
        C_re = (lambda *s: torch.randn(*s, dtype=torch.bfloat16) * 0.1)(D, N)
        C_im = (lambda *s: torch.randn(*s, dtype=torch.bfloat16) * 0.1)(D, N)
        log_dt = (lambda *s: torch.randn(*s, dtype=torch.bfloat16) * 0.1)(D) * 0.5 - 4.0
        Dskip = (lambda *s: torch.randn(*s, dtype=torch.bfloat16) * 0.1)(D)
        return {'u': _w((lambda *s: torch.randn(*s, dtype=torch.bfloat16) * 0.05)(B, L, D)), 'A_re': _w(A_re), 'A_im': _w(A_im), 'B_re': _w(B_re), 'B_im': _w(B_im), 'C_re': _w(C_re), 'C_im': _w(C_im), 'log_dt': _w(log_dt), 'Dskip': _w(Dskip)}
    _A_in = _A_setup()

    def _B_setup():
        B, S, D, F = (1, 2048, 448, 1792)
        return {'x': _w((lambda *s: torch.randn(*s, dtype=torch.bfloat16) * 0.05)(B, S, D)), 'w_gate': _w((lambda *s: torch.randn(*s, dtype=torch.bfloat16) * 1.6)(D, F) * D ** (-0.5)), 'w_up': _w((lambda *s: torch.randn(*s, dtype=torch.bfloat16) * 1.6)(D, F) * D ** (-0.5)), 'w_down': _w((lambda *s: torch.randn(*s, dtype=torch.bfloat16) * 1.6)(F, D) * F ** (-0.5))}
    _B_in = _B_setup()
    return {'A_u': _A_in['u'], 'A_A_re': _A_in['A_re'], 'A_A_im': _A_in['A_im'], 'A_B_re': _A_in['B_re'], 'A_B_im': _A_in['B_im'], 'A_C_re': _A_in['C_re'], 'A_C_im': _A_in['C_im'], 'A_log_dt': _A_in['log_dt'], 'A_Dskip': _A_in['Dskip'], 'B_x': _B_in['x'], 'B_w_gate': _B_in['w_gate'], 'B_w_up': _B_in['w_up'], 'B_w_down': _B_in['w_down'], 'c': _w((lambda *s: torch.randn(*s, dtype=torch.bfloat16) * 0.2)(1, 448) * 8.0), 'w_mod': _w((lambda *s: torch.randn(*s, dtype=torch.bfloat16) * 0.2)(448, 6 * 448) * 448 ** (-0.5) * 8.0)}

def reference(A_u, A_A_re, A_A_im, A_B_re, A_B_im, A_C_re, A_C_im, A_log_dt, A_Dskip, B_x, B_w_gate, B_w_up, B_w_down, c, w_mod):

    def _A_ref(u, A_re, A_im, B_re, B_im, C_re, C_im, log_dt, Dskip):
        Bsz, L, D = u.shape
        dt = torch.exp(log_dt)[:, None]
        A = A_re + 1j * A_im
        Bc = B_re + 1j * B_im
        Cc = C_re + 1j * C_im
        logdA = dt * A
        dB = dt * Bc
        l = (lambda x, dt: x.to(dt))(torch.arange(L), A_re.dtype)
        P = torch.exp(logdA[:, :, None] * l[None, None, :])
        K = torch.einsum('dn,dnl,dn->dl', Cc, P, dB).real
        L2 = 2 * L
        Kp = _pad(K, ((0, 0), (0, L)))
        up = (lambda x, *d: x.permute(*d))(u, 0, 2, 1)
        up = _pad(up, ((0, 0), (0, 0), (0, L)))
        Kf = (lambda x, axes=(-2, -1): torch.fft.rfft2(x.float(), dim=axes))(Kp[:, None, :], axes=(-2, -1))
        Uf = (lambda x, axes=(-2, -1): torch.fft.rfft2(x.float(), dim=axes))(up[:, :, None, :], axes=(-2, -1))
        Yf = Uf * Kf[None, :, :, :]
        y = (lambda x, s, axes=(-2, -1): torch.fft.irfft2(x, s=s, dim=axes).to(torch.bfloat16))(Yf, (1, L2), axes=(-2, -1))[:, :, 0, :L]
        y = (lambda x, *d: x.permute(*d))(y, 0, 2, 1) + u * Dskip[None, None, :]
        return y

    def _B_ref(x, w_gate, w_up, w_down):
        g = torch.einsum('bsd,df->bsf', x, w_gate)
        u = torch.einsum('bsd,df->bsf', x, w_up)
        h = g * u
        return torch.einsum('bsf,fd->bsd', h, w_down)
    x = A_u
    mod = (_silu(c) @ w_mod).reshape(c.shape[0], 6, 1, -1)
    sa, ca, ga, sb, cb, gb = (mod[:, 0].clone(), mod[:, 1].clone(), mod[:, 2].clone(), mod[:, 3].clone(), mod[:, 4].clone(), mod[:, 5].clone())
    h = _layernorm(x, axis=-1) * (1 + ca) + sa
    x = x + ga * _A_ref(h, A_A_re, A_A_im, A_B_re, A_B_im, A_C_re, A_C_im, A_log_dt, A_Dskip)
    h = _layernorm(x, axis=-1) * (1 + cb) + sb
    x = x + gb * _B_ref(h, B_w_gate, B_w_up, B_w_down)
    return x
```

```cpp
#include <hip/hip_runtime.h>
#include <hip/hip_bf16.h>
#include <stdint.h>

typedef unsigned short u16;
#define L_ 2048
#define D_ 448
#define N_ 256
#define F_ 1792

__device__ __forceinline__ float bf2f(u16 v){ return __uint_as_float(((uint32_t)v)<<16); }
__device__ __forceinline__ u16 f2bf(float f){ uint32_t u=__float_as_uint(f); u += 0x7FFF + ((u>>16)&1); return (u16)(u>>16); }
__device__ __forceinline__ float rbf(float f){ return bf2f(f2bf(f)); }

__global__ void k_mod(const u16* c, const u16* wmod, float* mod){
  int j = blockIdx.x*blockDim.x+threadIdx.x;
  if(j>=6*D_) return;
  float acc=0;
  for(int k=0;k<D_;k++){ float x=bf2f(c[k]); float s=rbf(x/(1.f+expf(-x))); acc += s*bf2f(wmod[k*6*D_+j]); }
  mod[j]=rbf(acc);
}
__global__ void k_ln(const u16* x, const float* shift, const float* scale, u16* h){
  int l=blockIdx.x; int t=threadIdx.x;
  float s=0; for(int d=t;d<D_;d+=64) s+=bf2f(x[l*D_+d]);
  for(int o=32;o;o>>=1) s+=__shfl_xor(s,o);
  float mf=s/D_; float m=rbf(mf);
  float v=0; for(int d=t;d<D_;d+=64){ float e=bf2f(x[l*D_+d])-mf; v+=e*e; }
  for(int o=32;o;o>>=1) v+=__shfl_xor(v,o);
  v=rbf(v/D_);
  float sd=rbf(sqrtf(rbf(v+1e-5f)));
  for(int d=t;d<D_;d+=64){ float e=rbf(bf2f(x[l*D_+d])-m); e=rbf(e/sd); e=rbf(e*rbf(1.f+scale[d])); e=rbf(e+shift[d]); h[l*D_+d]=f2bf(e);}
}
__global__ void k_kgen(const u16* Are,const u16* Aim,const u16* Bre,const u16* Bim,const u16* Cre,const u16* Cim,const u16* logdt,float* K){
  int d=blockIdx.y; int l=blockIdx.x*blockDim.x+threadIdx.x;
  float dt=rbf(expf(bf2f(logdt[d])));
  float lf=rbf((float)l);
  float acc=0;
  for(int n=0;n<N_;n++){
    float ar=dt*bf2f(Are[d*N_+n]), ai=dt*bf2f(Aim[d*N_+n]);
    float br=dt*bf2f(Bre[d*N_+n]), bi=dt*bf2f(Bim[d*N_+n]);
    float cr=bf2f(Cre[d*N_+n]), ci=bf2f(Cim[d*N_+n]);
    float e=expf(ar*lf); float s,c; sincosf(ai*lf,&s,&c);
    float pr=e*c, pi=e*s;
    float wr=cr*br-ci*bi, wi=cr*bi+ci*br;
    acc += wr*pr-wi*pi;
  }
  K[d*L_+l]=acc;
}
__global__ void k_conv(const u16* x,const u16* h,const float* K,const u16* Dskip,const float* ga,u16* x1){
  int d=blockIdx.y; int l=blockIdx.x*blockDim.x+threadIdx.x;
  float acc=0;
  for(int j=0;j<=l;j++) acc+=K[d*L_+j]*bf2f(h[(l-j)*D_+d]);
  float y=rbf(acc);
  float hd=rbf(bf2f(h[l*D_+d])*bf2f(Dskip[d]));
  y=rbf(y+hd);
  x1[l*D_+d]=f2bf(bf2f(x[l*D_+d])+rbf(ga[d]*y));
}
__global__ void k_gu(const u16* h,const u16* wg,const u16* wu,u16* H){
  int f=blockIdx.x*blockDim.x+threadIdx.x; int l=blockIdx.y;
  float g=0,u=0;
  for(int k=0;k<D_;k++){ float a=bf2f(h[l*D_+k]); g+=a*bf2f(wg[k*F_+f]); u+=a*bf2f(wu[k*F_+f]); }
  H[l*F_+f]=f2bf(rbf(g)*rbf(u));
}
__global__ void k_down(const u16* H,const u16* wd,const u16* x1,const float* gb,u16* out){
  int d=blockIdx.x*blockDim.x+threadIdx.x; int l=blockIdx.y;
  float a=0;
  for(int k=0;k<F_;k++) a+=bf2f(H[l*F_+k])*bf2f(wd[k*D_+d]);
  out[l*D_+d]=f2bf(bf2f(x1[l*D_+d])+rbf(gb[d]*rbf(a)));
}

extern "C" void kernel_launch(void* const* d_in, const int* in_sizes, int n_in,
                              void* d_out, int out_size, void* d_ws, size_t ws_size,
                              hipStream_t stream) {
  const u16* x=(const u16*)d_in[0];
  const u16 *Are=(const u16*)d_in[1],*Aim=(const u16*)d_in[2],*Bre=(const u16*)d_in[3],*Bim=(const u16*)d_in[4],*Cre=(const u16*)d_in[5],*Cim=(const u16*)d_in[6];
  const u16 *logdt=(const u16*)d_in[7],*Dskip=(const u16*)d_in[8];
  const u16 *wg=(const u16*)d_in[10],*wu=(const u16*)d_in[11],*wd=(const u16*)d_in[12],*c=(const u16*)d_in[13],*wmod=(const u16*)d_in[14];
  char* ws=(char*)d_ws;
  float* mod=(float*)ws; ws+=16384;
  u16* h=(u16*)ws; ws+=L_*D_*2;
  float* K=(float*)ws; ws+=L_*D_*4;
  u16* x1=(u16*)ws; ws+=L_*D_*2;
  u16* h2=(u16*)ws; ws+=L_*D_*2;
  u16* H=(u16*)ws; ws+=L_*F_*2;
  k_mod<<<(6*D_+255)/256,256,0,stream>>>(c,wmod,mod);
  k_ln<<<L_,64,0,stream>>>(x,mod+0*D_,mod+1*D_,h);
  k_kgen<<<dim3(L_/256,D_),256,0,stream>>>(Are,Aim,Bre,Bim,Cre,Cim,logdt,K);
  k_conv<<<dim3(L_/256,D_),256,0,stream>>>(x,h,K,Dskip,mod+2*D_,x1);
  k_ln<<<L_,64,0,stream>>>(x1,mod+3*D_,mod+4*D_,h2);
  k_gu<<<dim3(F_/256,L_),256,0,stream>>>(h2,wg,wu,H);
  k_down<<<dim3(D_/64,L_),64,0,stream>>>(H,wd,x1,mod+5*D_,(u16*)d_out);
}
```

```cpp
#include <hip/hip_runtime.h>
#include <stdint.h>

namespace mk {

typedef unsigned short u16;
typedef unsigned u32;
typedef u32 v4u __attribute__((ext_vector_type(4)));
typedef u32 v2u __attribute__((ext_vector_type(2)));
typedef float f2 __attribute__((ext_vector_type(2)));
typedef float f32x4 __attribute__((ext_vector_type(4)));
typedef float f32x16 __attribute__((ext_vector_type(16)));
typedef _Float16 h2 __attribute__((ext_vector_type(2)));
typedef _Float16 h8 __attribute__((ext_vector_type(8)));
typedef __bf16 b2 __attribute__((ext_vector_type(2)));
typedef __bf16 b8 __attribute__((ext_vector_type(8)));
typedef short s4 __attribute__((ext_vector_type(4)));
#define LAS __attribute__((address_space(3)))
#define GAS __attribute__((address_space(1)))
typedef GAS u32 gu32;
#define RLX_AGENT __ATOMIC_RELAXED, __HIP_MEMORY_SCOPE_AGENT

constexpr int L = 2048, D = 448, NS = 256, F = 1792;
constexpr int NWG = 256, NTHR = 512;
constexpr int SC1 = 16;

constexpr int WS_EPOCH = 0;
constexpr int WS_CNT = 4096;
constexpr int NCNT = 288;
constexpr int WS_STAT1 = WS_CNT + NCNT * 256;
constexpr int WS_MODG = WS_STAT1 + 256 * 128;
constexpr int WS_CSB = WS_MODG + 2688 * 8;
constexpr int WS_STAT2 = WS_CSB + 112 * 64 * 8;
constexpr int WS_XT8 = WS_STAT2 + 2048 * 8;
constexpr int WS_W1F = WS_XT8 + 256 * 448 * 16;
constexpr int WS_WDF = WS_W1F + 112 * 28 * 1024;
constexpr int WS_X1T = WS_WDF + 14 * 112 * 1024;
constexpr int WS_X1S = WS_X1T + 32 * 448 * 128;
constexpr int WS_HF = WS_X1S + 32 * 448 * 128;
constexpr int WS_DBG = WS_HF + 64 * 112 * 1024;
constexpr int WS_END = WS_DBG + (8 << 20);
static_assert(WS_STAT1 % 128 == 0 && WS_MODG % 128 == 0 && WS_CSB % 128 == 0 && WS_STAT2 % 128 == 0 && WS_XT8 % 128 == 0, "align");

constexpr int PQ_ROW = 1056, PQ_CH = 57 * PQ_ROW;
constexpr int LD_PQ = 0;
constexpr int LD_KC = 0;
constexpr int KC_CP = 4160, KC_CH = 4 * KC_CP;
constexpr int LD_EX = 33280;
constexpr int LD_XB = 0;
constexpr int XB_SZ = 448 * 128;
constexpr int LD_RED = 0;
constexpr int RED_ROW = 272, RED_W = 32 * RED_ROW;
constexpr int LD_RES = 8 * RED_W;
constexpr int LD_KD = 120384;
constexpr int KD_CH = 784 * 2;
constexpr int LD_U = LD_KD + 2 * KD_CH;
constexpr int U_CH = 2304 * 2;
constexpr int LD_XS = LD_U + 2 * U_CH;
constexpr int LD_XROW = 120384;
constexpr int LD_WSTG = LD_XROW + 8 * 960;
constexpr int WSTG_ROW = 464, WSTG_M = 16 * WSTG_ROW;
constexpr int LD_SMALL = 150400;
constexpr int LD_STAT = LD_SMALL;
constexpr int LD_WMAX = LD_SMALL + 32;
constexpr int LD_FLAG = LD_SMALL + 64;
constexpr int LD_CSL = LD_SMALL + 128;
constexpr int LD_CV = LD_CSL + 2048;
constexpr int LD_END = LD_CV + 2 * 448 * 2;
constexpr int LDS_BYTES = 156 * 1024;
static_assert(LD_XS + 8192 <= LD_SMALL && LD_WSTG + 3 * WSTG_M <= LD_SMALL && LD_END <= LDS_BYTES, "lds");
static_assert(LD_RES + 4096 <= LD_KD, "lds red");

struct Params {
  const u16 *x, *Are, *Aim, *Bre, *Bim, *Cre, *Cim, *logdt, *Dsk, *wg, *wu, *wd, *cvec, *wmod;
  u16 *out;
  unsigned char *ws;
};

struct KTab { u16 v[256][16]; };
constexpr int bf16_round_int(int j) {
  if (j < 256) return j;
  int sh = 0; int t = j; while (t >= 256) { t >>= 1; sh++; }
  int q = j >> sh, rem = j & ((1 << sh) - 1), half = 1 << (sh - 1);
  if (rem > half || (rem == half && (q & 1))) q++;
  return q << sh;
}
constexpr int kd_index(int j) {
  if (j < 0) return 768;
  int l = bf16_round_int(j);
  if (l >= 2048) return 40 * 16;
  int k = l < 256 ? 0 : (l < 512 ? 1 : (l < 1024 ? 2 : 3));
  int base = k ? (128 << k) : 0;
  int rem = l - base;
  int a = rem >> 7, b = (rem >> 3) & 15, c = (rem & 7) >> k;
  int row = (k ? 8 + 8 * k : 0) + a * (8 >> k) + c;
  return row * 16 + b;
}
constexpr KTab make_ktab() {
  KTab t{};
  for (int i = 0; i < 256; i++)
    for (int j = 0; j < 16; j++) t.v[i][j] = (u16)(j < 11 ? kd_index(8 * i - 3 + j) : 768);
  return t;
}
__device__ const KTab g_ktab = make_ktab();

__device__ __forceinline__ float asf(u32 v) { return __builtin_bit_cast(float, v); }
__device__ __forceinline__ h2 ash2(u32 v) { return __builtin_bit_cast(h2, v); }
__device__ __forceinline__ float bf2f(u32 v) { return __builtin_bit_cast(float, v << 16); }
__device__ __forceinline__ float bflo(u32 v) { return __builtin_bit_cast(float, v << 16); }
__device__ __forceinline__ float bfhi(u32 v) { return __builtin_bit_cast(float, v & 0xFFFF0000u); }
__device__ __forceinline__ u32 pkbf(float a, float b) {
  return __builtin_bit_cast(u32, __builtin_convertvector(f2{a, b}, b2));
}
__device__ __forceinline__ u32 pkh(float a, float b) {
  return __builtin_bit_cast(u32, __builtin_convertvector(f2{a, b}, h2));
}
__device__ __forceinline__ float rbf(float a) { return bflo(pkbf(a, a)); }
__device__ __forceinline__ f2 cmul(f2 a, f2 b) {
  f2 t = f2{a.x, a.x} * b;
  return __builtin_elementwise_fma(f2{a.y, a.y}, f2{-b.y, b.x}, t);
}
template <int CTRL, int RM> __device__ __forceinline__ float dpp0(float v) {
  return __builtin_bit_cast(float, __builtin_amdgcn_update_dpp(0, __builtin_bit_cast(int, v), CTRL, RM, 0xF, true));
}
__device__ __forceinline__ float wave_sum(float v) {
  v += dpp0<0x111, 0xF>(v);
  v += dpp0<0x112, 0xF>(v);
  v += dpp0<0x114, 0xF>(v);
  v += dpp0<0x118, 0xF>(v);
  v += dpp0<0x142, 0xA>(v);
  v += dpp0<0x143, 0xC>(v);
  return __builtin_bit_cast(float, __builtin_amdgcn_readlane(__builtin_bit_cast(int, v), 63));
}
__device__ __forceinline__ float wave_max_nonneg(float v) {
  v = fmaxf(v, dpp0<0x111, 0xF>(v));
  v = fmaxf(v, dpp0<0x112, 0xF>(v));
  v = fmaxf(v, dpp0<0x114, 0xF>(v));
  v = fmaxf(v, dpp0<0x118, 0xF>(v));
  v = fmaxf(v, dpp0<0x142, 0xA>(v));
  v = fmaxf(v, dpp0<0x143, 0xC>(v));
  return __builtin_bit_cast(float, __builtin_amdgcn_readlane(__builtin_bit_cast(int, v), 63));
}
__device__ __forceinline__ v2u trread(LAS unsigned char *p) {
  return __builtin_bit_cast(v2u, __builtin_amdgcn_ds_read_tr16_b64_v4i16((LAS s4 *)p));
}
__device__ __forceinline__ v4u tr8(LAS unsigned char *p0, LAS unsigned char *p1) {
  v2u a = trread(p0), b = trread(p1);
  return v4u{a.x, a.y, b.x, b.y};
}
__device__ __forceinline__ f32x4 mfma16h(v4u a, v4u b, f32x4 c) {
  return __builtin_amdgcn_mfma_f32_16x16x32_f16(__builtin_bit_cast(h8, a), __builtin_bit_cast(h8, b), c, 0, 0, 0);
}
__device__ __forceinline__ f32x16 mfma32b(v4u a, v4u b, f32x16 c) {
  return __builtin_amdgcn_mfma_f32_32x32x16_bf16(__builtin_bit_cast(b8, a), __builtin_bit_cast(b8, b), c, 0, 0, 0);
}
#define BAR() asm volatile("s_waitcnt lgkmcnt(0)\n\ts_barrier" ::: "memory")
#define DRAIN() asm volatile("s_waitcnt vmcnt(0)" ::: "memory")
#define PIN() __builtin_amdgcn_sched_barrier(0)
typedef __amdgpu_buffer_rsrc_t rsrc_t;
__device__ __forceinline__ v4u ld16(rsrc_t rs, int off) { return __builtin_amdgcn_raw_buffer_load_b128(rs, off, 0, SC1); }
__device__ __forceinline__ v2u ld8(rsrc_t rs, int off) { return __builtin_amdgcn_raw_buffer_load_b64(rs, off, 0, SC1); }
__device__ __forceinline__ void st16(rsrc_t rs, int off, v4u v) { __builtin_amdgcn_raw_buffer_store_b128(v, rs, off, 0, SC1); }
__device__ __forceinline__ void st8(rsrc_t rs, int off, v2u v) { __builtin_amdgcn_raw_buffer_store_b64(v, rs, off, 0, SC1); }
__device__ __forceinline__ void st4(rsrc_t rs, int off, u32 v) { __builtin_amdgcn_raw_buffer_store_b32(v, rs, off, 0, SC1); }

constexpr unsigned SPIN_CAP = 1u << 14;

#define STAMP(i)

__device__ __forceinline__ void poll_counters(const Params &p, int idx0, int ns, u32 want, int lane) {
  gu32 *cw = (gu32 *)(p.ws + WS_CNT) + (idx0 + (lane < ns ? lane : 0)) * 64;
  for (unsigned sp = 0; sp < SPIN_CAP; sp++) {
    u32 v = __hip_atomic_load(cw, RLX_AGENT);
    if (__all(v >= want)) break;
    __builtin_amdgcn_s_sleep(1);
  }
  __builtin_amdgcn_fence(__ATOMIC_ACQUIRE, "wavefront");
}
__device__ __forceinline__ u32 wait_granule(rsrc_t rs, int off, u32 tag, bool active) {
  v2u g = {0, tag};
  for (unsigned sp = 0; sp < SPIN_CAP; sp++) {
    if (active) g = ld8(rs, off);
    if (__all(g.y == tag)) break;
    __builtin_amdgcn_s_sleep(1);
  }
  __builtin_amdgcn_fence(__ATOMIC_ACQUIRE, "wavefront");
  return g.x;
}

template <int ROW> __device__ __forceinline__ void pqw4(u32 m0v, f2 a, f2 b, f2 c, f2 d) {
  const u32 h0 = pkh(a.x, a.y), h1 = pkh(b.x, b.y), h2_ = pkh(c.x, c.y), h3 = pkh(d.x, d.y);
  asm volatile("s_mov_b32 m0, %4\n\ts_nop 0\n\tds_write_addtid_b32 %0 offset:%c5\n\tds_write_addtid_b32 %1 offset:%c6\n\t"
               "ds_write_addtid_b32 %2 offset:%c7\n\tds_write_addtid_b32 %3 offset:%c8"
               ::"v"(h0), "v"(h1), "v"(h2_), "v"(h3), "s"(m0v), "i"(ROW * PQ_ROW), "i"((ROW + 1) * PQ_ROW),
               "i"((ROW + 2) * PQ_ROW), "i"((ROW + 3) * PQ_ROW) : "memory");
}
template <int ROW> __device__ __forceinline__ void pqw1(u32 m0v, f2 a) {
  const u32 h0 = pkh(a.x, a.y);
  asm volatile("s_mov_b32 m0, %1\n\ts_nop 0\n\tds_write_addtid_b32 %0 offset:%c2" ::"v"(h0), "s"(m0v), "i"(ROW * PQ_ROW) : "memory");
}
__device__ __forceinline__ f2 zpow(float al, float rv) {
  float e = __builtin_amdgcn_exp2f(al);
  float r = __builtin_amdgcn_fractf(rv);
  return f2{e * __builtin_amdgcn_cosf(r), e * __builtin_amdgcn_sinf(r)};
}

__global__ void __launch_bounds__(NTHR, 2) k_main(Params p) {
  extern __shared__ __attribute__((aligned(16))) unsigned char smem[];
  LAS unsigned char *S = (LAS unsigned char *)smem;
  const int tid = threadIdx.x, lane = tid & 63, b = blockIdx.x;
  const int w = __builtin_amdgcn_readfirstlane(tid >> 6);
  const int g = b & 7, r = b >> 3;
  const bool is_main = r < 28;
  const int m = 28 * g + r;
  const int sidx = 4 * g + r - 28;
  const rsrc_t rs = __builtin_amdgcn_make_buffer_rsrc((void *)p.ws, 0, WS_END, 0x00020000);
  STAMP(0);

  u32 pf = 0;
  {
    unsigned long long pc;
    asm volatile("s_getpc_b64 %0" : "=s"(pc));
    const GAS u32 *cp = (const GAS u32 *)((pc & ~127ull) + (unsigned)tid * 128u);
    if (tid < 256) pf = *cp;
  }
  const u32 epoch = ld8(rs, WS_EPOCH).x;
  v4u xr = {0, 0, 0, 0};
  if (lane < 56) xr = *(const GAS v4u *)(p.x + (8 * b + w) * D + lane * 8);
  u32 pAre = 0, pAim = 0, pBre = 0, pBim = 0, pCre = 0, pCim = 0, pLdt = 0, pDsk = 0;
  v4u ws0 = {0, 0, 0, 0}, ws1 = ws0, ws2 = ws0;
  v4u kt0 = ws0, kt1 = ws0;
  v4u wm[11];
  u32 cv[11];
  const int cch = tid >> 8;
  const int t8 = tid & 255;
  const int dch = 2 * m + cch;
  if (is_main) {
    const int o = dch * NS + t8;
    pAre = p.Are[o]; pAim = p.Aim[o]; pBre = p.Bre[o]; pBim = p.Bim[o]; pCre = p.Cre[o]; pCim = p.Cim[o];
    pLdt = p.logdt[dch]; pDsk = p.Dsk[dch];
    if (tid < 448) {
      const int row = tid / 28, c = tid % 28;
      const int s = m >> 3, f0 = 224 * (m & 7);
      ws0 = *(const GAS v4u *)(p.wg + (16 * s + row) * F + f0 + 8 * c);
      ws1 = *(const GAS v4u *)(p.wu + (16 * s + row) * F + f0 + 8 * c);
      ws2 = *(const GAS v4u *)(p.wd + (16 * (m >> 1) + row) * D + 224 * (m & 1) + 8 * c);
    }
    kt0 = *(const GAS v4u *)(&g_ktab.v[t8][0]);
    kt1 = *(const GAS v4u *)(&g_ktab.v[t8][8]);
  } else if (sidx < 28) {
    const int cg = tid % 12, rg = tid / 12;
#pragma unroll
    for (int i = 0; i < 11; i++) {
      const int k = rg + 42 * i;
      const bool ok = tid < 504 && k < D;
      wm[i] = v4u{0, 0, 0, 0};
      cv[i] = 0;
      if (ok) { wm[i] = *(const GAS v4u *)(p.wmod + k * (6 * D) + 96 * sidx + 8 * cg); cv[i] = p.cvec[k]; }
    }
  }
  PIN();
  const u32 tag = __builtin_amdgcn_readfirstlane(epoch) + 1u;

  {
    float xv[8] = {bflo(xr.x), bfhi(xr.x), bflo(xr.y), bfhi(xr.y), bflo(xr.z), bfhi(xr.z), bflo(xr.w), bfhi(xr.w)};
    float s = 0.f;
#pragma unroll
    for (int i = 0; i < 8; i++) s += xv[i];
    const float mean = wave_sum(s) * (1.0f / D);
    float q = 0.f;
#pragma unroll
    for (int i = 0; i < 8; i++) { float e = xv[i] - mean; q += e * e; }
    if (lane >= 56) q = 0.f;
    const float var = rbf(wave_sum(q) * (1.0f / D));
    const float sd = rbf(__builtin_sqrtf(rbf(var + 1e-5f)));
    if (lane == 0) *(LAS u32 *)(S + LD_STAT + w * 4) = pkbf(mean, sd);
    if (lane < 56) *(LAS v4u *)(S + LD_XROW + w * 960 + lane * 16) = xr;
    if (is_main && tid < 448) {
      const int row = tid / 28, c = tid % 28;
      *(LAS v4u *)(S + LD_WSTG + 0 * WSTG_M + row * WSTG_ROW + c * 16) = ws0;
      *(LAS v4u *)(S + LD_WSTG + 1 * WSTG_M + row * WSTG_ROW + c * 16) = ws1;
      *(LAS v4u *)(S + LD_WSTG + 2 * WSTG_M + row * WSTG_ROW + c * 16) = ws2;
    }
  }
  STAMP(1);
  BAR();
  {
    const int G = lane >> 4, e = (lane >> 2) & 3, pp = lane & 3;
    if (w < 7) {
      LAS unsigned char *a = S + LD_XROW + e * 960 + (64 * w + 16 * G + 4 * pp) * 2;
      st16(rs, WS_XT8 + (b * D + 64 * w + lane) * 16, tr8(a, a + 4 * 960));
    }
    if (is_main) {
      const int h = G >> 1;
#pragma unroll
      for (int jj = 0; jj < 2; jj++) {
        const int j = w + 8 * jj;
        if (j < 14) {
          LAS unsigned char *a = S + LD_WSTG + (G & 1) * WSTG_M + (8 * h + e) * WSTG_ROW + (16 * j + 4 * pp) * 2;
          st16(rs, WS_W1F + ((14 * (m & 7) + j) * 28 + (m >> 3)) * 1024 + lane * 16, tr8(a, a + 4 * WSTG_ROW));
        }
      }
      if (w < 7) {
        LAS unsigned char *a = S + LD_WSTG + 2 * WSTG_M + (4 * h + e) * WSTG_ROW + (32 * w + 16 * (G & 1) + 4 * pp) * 2;
        st16(rs, WS_WDF + ((7 * (m & 1) + w) * 112 + (m >> 1)) * 1024 + lane * 16, tr8(a, a + 8 * WSTG_ROW));
      }
    }
    if (tid == 0) st4(rs, WS_CNT + b * 256, 0u);
    if (tid == 64 && b < NCNT - 256) st4(rs, WS_CNT + (256 + b) * 256, 0u);
  }
  STAMP(2);

  if (is_main) {
    const float dt = rbf(expf(bf2f(pLdt)));
    const float al = dt * bf2f(pAre) * 1.44269504088896f;
    const float rv = dt * bf2f(pAim) * 0.159154943091895f;
    const f2 Wc = cmul(f2{bf2f(pCre), bf2f(pCim)}, f2{dt * bf2f(pBre), dt * bf2f(pBim)});
    {
      const float mx = wave_max_nonneg(fmaxf(fabsf(Wc.x), fabsf(Wc.y)));
      if (lane == 0) *(LAS float *)(S + LD_WMAX + w * 4) = mx;
    }
    const u32 m0v = (u32)(LD_PQ + (w >> 2) * PQ_CH + (w & 3) * 256);
    const f2 p1 = zpow(al, rv), z8 = zpow(al * 8.f, rv * 8.f), Z1 = zpow(al * 128.f, rv * 128.f);
    const f2 p2 = cmul(p1, p1), p3 = cmul(p2, p1), p4 = cmul(p2, p2), p5 = cmul(p4, p1), p6 = cmul(p4, p2), p7 = cmul(p4, p3);
    pqw4<0>(m0v, f2{1.f, 0.f}, p1, p2, p3);
    pqw4<4>(m0v, p4, p5, p6, p7);
    pqw4<8>(m0v, Z1, cmul(Z1, p1), cmul(Z1, p2), cmul(Z1, p3));
    pqw4<12>(m0v, cmul(Z1, p4), cmul(Z1, p5), cmul(Z1, p6), cmul(Z1, p7));
    const f2 Z2 = cmul(Z1, Z1), Z3 = cmul(Z2, Z1), Z4 = cmul(Z2, Z2);
    pqw4<16>(m0v, Z2, cmul(Z2, p2), cmul(Z2, p4), cmul(Z2, p6));
    pqw4<20>(m0v, Z3, cmul(Z3, p2), cmul(Z3, p4), cmul(Z3, p6));
    const f2 Z5 = cmul(Z4, Z1), Z6 = cmul(Z4, Z2), Z7 = cmul(Z4, Z3), Z8 = cmul(Z4, Z4);
    pqw4<24>(m0v, Z4, cmul(Z4, p4), Z5, cmul(Z5, p4));
    pqw4<28>(m0v, Z6, cmul(Z6, p4), Z7, cmul(Z7, p4));
    pqw4<32>(m0v, Z8, cmul(Z8, Z1), cmul(Z8, Z2), cmul(Z8, Z3));
    pqw4<36>(m0v, cmul(Z8, Z4), cmul(Z8, Z5), cmul(Z8, Z6), cmul(Z8, Z7));
    pqw1<40>(m0v, cmul(Z8, Z8));
    STAMP(3);
    DRAIN();
    BAR();
    if (tid < 8) st8(rs, WS_STAT1 + b * 128 + tid * 8, v2u{*(LAS u32 *)(S + LD_STAT + tid * 4), tag});
    float mx = 0.f;
#pragma unroll
    for (int i = 0; i < 4; i++) mx = fmaxf(mx, *(LAS float *)(S + LD_WMAX + ((w & 4) + i) * 4));
    int eb = (int)((__builtin_bit_cast(u32, mx) >> 23) & 0xFF);
    int sb_ = 267 - eb; sb_ = sb_ > 254 ? 254 : (sb_ < 1 ? 1 : sb_);
    const float sQ = __builtin_bit_cast(float, (u32)sb_ << 23);
    {
      const f2 z16 = cmul(z8, z8), z24 = cmul(z16, z8), z32 = cmul(z16, z16);
      f2 q0 = Wc * sQ, q1 = cmul(q0, z8), q2 = cmul(q0, z16), q3 = cmul(q0, z24);
#define NEGI(q) f2{(q).x, -(q).y}
      pqw4<41>(m0v, NEGI(q0), NEGI(q1), NEGI(q2), NEGI(q3));
      q0 = cmul(q0, z32); q1 = cmul(q1, z32); q2 = cmul(q2, z32); q3 = cmul(q3, z32);
      pqw4<45>(m0v, NEGI(q0), NEGI(q1), NEGI(q2), NEGI(q3));
      q0 = cmul(q0, z32); q1 = cmul(q1, z32); q2 = cmul(q2, z32); q3 = cmul(q3, z32);
      pqw4<49>(m0v, NEGI(q0), NEGI(q1), NEGI(q2), NEGI(q3));
      q0 = cmul(q0, z32); q1 = cmul(q1, z32); q2 = cmul(q2, z32); q3 = cmul(q3, z32);
      pqw4<53>(m0v, NEGI(q0), NEGI(q1), NEGI(q2), NEGI(q3));
#undef NEGI
    }
    STAMP(4);
    BAR();
    v4u sg[4];
    v2u mg[4];
#pragma unroll
    for (int i = 0; i < 4; i++) sg[i] = ld16(rs, WS_STAT1 + t8 * 128 + i * 16);
    mg[0] = ld8(rs, WS_MODG + (0 * D + dch) * 8);
    mg[1] = ld8(rs, WS_MODG + (1 * D + dch) * 8);
    mg[2] = ld8(rs, WS_MODG + (2 * D + dch) * 8);
    mg[3] = ld8(rs, WS_MODG + (4 * D + dch) * 8);
    PIN();
    {
      const int c = w >> 2, tile = w & 3;
      if (tile < 3) {
        LAS unsigned char *pa = S + LD_PQ + c * PQ_CH + (16 * tile + (lane & 15)) * PQ_ROW + (lane >> 4) * 16;
        LAS unsigned char *pb = S + LD_PQ + c * PQ_CH + (41 + (lane & 15)) * PQ_ROW + (lane >> 4) * 16;
        f32x4 acc = {0.f, 0.f, 0.f, 0.f};
#pragma unroll
        for (int ks = 0; ks < 16; ks++) acc = mfma16h(*(LAS v4u *)(pa + ks * 64), *(LAS v4u *)(pb + ks * 64), acc);
        LAS u16 *kd = (LAS u16 *)(S + LD_KD + c * KD_CH) + (16 * tile + 4 * (lane >> 4)) * 16 + (lane & 15);
#pragma unroll
        for (int i = 0; i < 4; i++) kd[i * 16] = (u16)(pkh(acc[i] * (1.0f / 256.0f), 0.f) & 0xFFFFu);
      } else if (lane < 16) {
        ((LAS u16 *)(S + LD_KD + c * KD_CH))[768 + lane] = 0;
      }
    }
    for (unsigned sp = 0; sp < SPIN_CAP; sp++) {
      bool ok = true;
#pragma unroll
      for (int i = 0; i < 4; i++) ok = ok && sg[i].y == tag && sg[i].w == tag && mg[i].y == tag;
      if (__all(ok)) break;
      __builtin_amdgcn_s_sleep(1);
#pragma unroll
      for (int i = 0; i < 4; i++) sg[i] = ld16(rs, WS_STAT1 + t8 * 128 + i * 16);
      mg[0] = ld8(rs, WS_MODG + (0 * D + dch) * 8);
      mg[1] = ld8(rs, WS_MODG + (1 * D + dch) * 8);
      mg[2] = ld8(rs, WS_MODG + (2 * D + dch) * 8);
      mg[3] = ld8(rs, WS_MODG + (4 * D + dch) * 8);
    }
    __builtin_amdgcn_fence(__ATOMIC_ACQUIRE, "wavefront");
    const v4u xt = ld16(rs, WS_XT8 + (t8 * D + dch) * 16);
    STAMP(5);
    BAR();
    if (b == 0 && tid == 0) st4(rs, WS_EPOCH, tag);
    {
      LAS u16 *kd = (LAS u16 *)(S + LD_KD + cch * KD_CH);
      u32 v[11];
      const u32 ix[6] = {kt0.x, kt0.y, kt0.z, kt0.w, kt1.x, kt1.y};
#pragma unroll
      for (int i = 0; i < 11; i++) v[i] = kd[(i & 1) ? (ix[i >> 1] >> 16) : (ix[i >> 1] & 0xFFFFu)];
      LAS unsigned char *kc = S + LD_KC + cch * KC_CH + (16 + 8 * t8) * 2;
#pragma unroll
      for (int e = 0; e < 4; e++) {
        v4u o;
        o.x = v[3 - e] | (v[4 - e] << 16);
        o.y = v[5 - e] | (v[6 - e] << 16);
        o.z = v[7 - e] | (v[8 - e] << 16);
        o.w = v[9 - e] | (v[10 - e] << 16);
        *(LAS v4u *)(kc + e * KC_CP) = o;
      }
      if (t8 < 8) *(LAS v4u *)(S + LD_KC + cch * KC_CH + (t8 >> 1) * KC_CP + (t8 & 1) * 16) = v4u{0, 0, 0, 0};
    }
    const float sa = asf(mg[0].x), ca = asf(mg[1].x);
    const float ga = asf(mg[2].x), cb = asf(mg[3].x);
    const float c1 = rbf(1.0f + ca), c2 = rbf(1.0f + cb);
    float sH;
    {
      const float bd = 21.5f * fabsf(c1) + fabsf(sa);
      int e2 = (int)((__builtin_bit_cast(u32, bd) >> 23) & 0xFF);
      int sbits = 127 + 14 - (e2 - 126); sbits = sbits > 254 ? 254 : (sbits < 1 ? 1 : sbits);
      sH = __builtin_bit_cast(float, (u32)sbits << 23);
    }
    {
      const u32 xq[4] = {xt.x, xt.y, xt.z, xt.w};
      u32 uo[4];
#pragma unroll
      for (int i = 0; i < 4; i++) {
        const u32 s0 = sg[i].x, s1 = sg[i].z;
        const float h0 = ((bflo(xq[i]) - bflo(s0)) * __builtin_amdgcn_rcpf(bfhi(s0))) * c1 + sa;
        const float h1 = ((bfhi(xq[i]) - bflo(s1)) * __builtin_amdgcn_rcpf(bfhi(s1))) * c1 + sa;
        uo[i] = pkh(h0 * sH, h1 * sH);
      }
      *(LAS v4u *)(S + LD_U + cch * U_CH + (256 + 8 * t8) * 2) = v4u{uo[0], uo[1], uo[2], uo[3]};
      *(LAS v4u *)(S + LD_XS + cch * 4096 + 16 * t8) = xt;
      if (t8 < 32) *(LAS v4u *)(S + LD_U + cch * U_CH + 16 * t8) = v4u{0, 0, 0, 0};
    }
    const float A1 = ga / (sQ * (1.0f / 256.0f)) / sH;
    const float A2 = ga * bf2f(pDsk) / sH;
    STAMP(6);
    BAR();

    v4u w1[28];
    const int fc = 7 * (g & 1) + r % 7, tt = 4 * (g >> 1) + r / 7;
    const int phi = 8 * fc + w;
    {
      const int c = w >> 2, pq = w & 3;
      const int a15 = lane & 15, q = lane >> 4, e = (lane >> 2) & 3, pp = lane & 3;
      LAS unsigned char *ka = S + LD_KC + c * KC_CH + e * KC_CP + (16 + 16 * (q >> 1) - 8 * (q & 1) + 4 * pp) * 2;
      LAS unsigned char *ub = S + LD_U + c * U_CH + 512 + 32 * a15 - 32 * (q >> 1) + 16 * (q & 1);
#pragma unroll
      for (int hf = 0; hf < 2; hf++) {
        f32x4 acc[4];
#pragma unroll
        for (int i = 0; i < 4; i++) acc[i] = f32x4{0.f, 0.f, 0.f, 0.f};
#pragma unroll 1
        for (int pi = 0; pi < 2; pi++) {
          const int pv = 2 * pq + pi;
          constexpr int NQ0 = 4, NQ1 = 8;
          v4u A[8], B[8];
#pragma unroll
          for (int i = 0; i < 8; i++)
            if (i < (hf ? NQ1 : NQ0)) {
              A[i] = tr8(ka + i * 512 + pv * 64, ka + i * 512 + pv * 64 - 8);
              B[i] = *(LAS v4u *)(ub + i * 512 - pv * 64);
            }
#pragma unroll
          for (int tl = 0; tl < 4; tl++)
#pragma unroll
            for (int qq = 0; qq < 8; qq++)
              if (qq <= 4 * hf + tl) acc[tl] = mfma16h(A[qq], B[4 * hf + tl - qq], acc[tl]);
        }
        STAMP(17 + 2 * hf);
        LAS unsigned char *ex = S + LD_EX + c * 16384;
#pragma unroll
        for (int tl = 0; tl < 4; tl++) *(LAS f32x4 *)(ex + (pq * 4 + tl) * 1024 + lane * 16) = acc[tl];
        if (hf == 1) asm volatile("s_waitcnt vmcnt(14)" ::: "memory");
        BAR();
        if (hf == 1 && tid < 8) __hip_atomic_fetch_add((gu32 *)(p.ws + WS_CNT) + (0 * 128 + tid * 16 + (m & 15)) * 64, 1u, RLX_AGENT);
        f32x4 y = *(LAS f32x4 *)(ex + (0 * 4 + pq) * 1024 + lane * 16);
#pragma unroll
        for (int s2 = 1; s2 < 4; s2++) y += *(LAS f32x4 *)(ex + (s2 * 4 + pq) * 1024 + lane * 16);
        const int t = 4 * hf + pq;
        const int l0 = 256 * t + 16 * a15 + 4 * q;
        const v2u hh = *(LAS v2u *)(S + LD_U + c * U_CH + (256 + l0) * 2);
        const v2u xx = *(LAS v2u *)(S + LD_XS + c * 4096 + l0 * 2);
        const h2 ha = ash2(hh.x), hb = ash2(hh.y);
        const float o0 = bflo(xx.x) + A1 * y[0] + A2 * (float)ha.x;
        const float o1 = bfhi(xx.x) + A1 * y[1] + A2 * (float)ha.y;
        const float o2 = bflo(xx.y) + A1 * y[2] + A2 * (float)hb.x;
        const float o3 = bfhi(xx.y) + A1 * y[3] + A2 * (float)hb.y;
        const u32 X0 = pkbf(o0, o1), X1 = pkbf(o2, o3);
        const u32 Y0 = pkbf(bflo(X0) * c2, bfhi(X0) * c2), Y1 = pkbf(bflo(X1) * c2, bfhi(X1) * c2);
        const auto r0 = __builtin_amdgcn_permlane16_swap(X0, Y0, false, false);
        const auto r1 = __builtin_amdgcn_permlane16_swap(X1, Y1, false, false);
        const int l8 = 256 * t + 16 * a15 + 8 * (q >> 1);
        const int dd = 2 * m + c;
        st16(rs, ((q & 1) ? WS_X1S : WS_X1T) + ((l8 >> 6) * D + dd) * 128 + (l8 & 63) * 2, v4u{r0[0], r1[0], r0[1], r1[1]});
        PIN();
        STAMP(18 + 4 * hf);
#pragma unroll
        for (int s = 14 * hf; s < 14 * hf + 14; s++) w1[s] = ld16(rs, WS_W1F + (phi * 28 + s) * 1024 + lane * 16);
        PIN();
        if (hf == 0) BAR();
      }
      asm volatile("s_waitcnt vmcnt(14)" ::: "memory");
      BAR();
      if (tid < 8) __hip_atomic_fetch_add((gu32 *)(p.ws + WS_CNT) + (1 * 128 + tid * 16 + (m & 15)) * 64, 1u, RLX_AGENT);
    }
    STAMP(7);

    const u32 csb = wait_granule(rs, WS_CSB + (phi * 64 + lane) * 8, tag, true);
    *(LAS u32 *)(S + LD_CSL + (w * 64 + lane) * 4) = csb;
    if (w == 0) poll_counters(p, 0 * 128 + g * 16, 16, 14u, lane);
    BAR();
    STAMP(8);
    v4u xs[7];
#pragma unroll
    for (int i = 0; i < 7; i++) xs[i] = ld16(rs, WS_X1S + tt * XB_SZ + (tid + 512 * i) * 16);
#pragma unroll
    for (int i = 0; i < 7; i++) {
      const int pi = tid + 512 * i, d = pi >> 3, c = pi & 7;
      *(LAS v4u *)(S + LD_XB + d * 128 + ((c ^ (((d >> 1) & 1) << 2)) * 16)) = xs[i];
    }
    BAR();
    STAMP(9);
    v4u wd[28];
    v2u sg2[2];
    const int dtile = r % 7, t2 = 4 * g + r / 7;
    {
      const int G = lane >> 4, e = (lane >> 2) & 3, pp = lane & 3, h = lane >> 5;
      const int sw = ((e >> 1) & 1) * 64;
      LAS unsigned char *bx0 = S + LD_XB + (8 * h + e) * 128 + ((0 + 32 * (G & 1) + 8 * pp) ^ sw);
      LAS unsigned char *bx1 = S + LD_XB + (8 * h + e) * 128 + ((64 + 32 * (G & 1) + 8 * pp) ^ sw);
#pragma unroll
      for (int hf = 0; hf < 2; hf++) {
        f32x16 acc[2];
#pragma unroll
        for (int j = 0; j < 2; j++)
#pragma unroll
          for (int i = 0; i < 16; i++) acc[j][i] = 0.f;
#pragma unroll
        for (int s = 0; s < 28; s++) {
          const int o = hf * XB_SZ + s * 2048;
          const v4u b0 = tr8(bx0 + o, bx0 + o + 512), b1 = tr8(bx1 + o, bx1 + o + 512);
          acc[0] = mfma32b(w1[s], b0, acc[0]);
          acc[1] = mfma32b(w1[s], b1, acc[1]);
          if (hf == 0 && s == 13) {
            if (w == 0) poll_counters(p, 1 * 128 + g * 16, 16, 14u, lane);
            BAR();
#pragma unroll
            for (int i = 0; i < 7; i++) xs[i] = ld16(rs, WS_X1S + (16 + tt) * XB_SZ + (tid + 512 * i) * 16);
            PIN();
          }
          if (s == 20) {
#pragma unroll
            for (int j = 0; j < 2; j++) sg2[j] = ld8(rs, WS_STAT2 + (64 * (16 * hf + tt) + 32 * j + (lane & 31)) * 8);
            PIN();
          }
          if (hf == 1 && s == 12) {
            asm volatile("s_waitcnt vmcnt(12)" ::: "memory");
            BAR();
            if (tid == 0) __hip_atomic_fetch_add((gu32 *)(p.ws + WS_CNT) + (256 + tt) * 64, 1u, RLX_AGENT);
          }
          if (hf == 1) {
            const int dl = s / 14, kk = s % 14;
            wd[s] = ld16(rs, WS_WDF + ((2 * dtile + dl) * 112 + 14 * w + kk) * 1024 + lane * 16);
          }
        }
        if (hf == 0) {
#pragma unroll
          for (int i = 0; i < 7; i++) {
            const int pi = tid + 512 * i, d = pi >> 3, c = pi & 7;
            *(LAS v4u *)(S + LD_XB + XB_SZ + d * 128 + ((c ^ (((d >> 1) & 1) << 2)) * 16)) = xs[i];
          }
        }
        const int T = 16 * hf + tt;
        for (unsigned sp = 0; sp < SPIN_CAP; sp++) {
          if (__all(sg2[0].y == tag && sg2[1].y == tag)) break;
          __builtin_amdgcn_s_sleep(1);
#pragma unroll
          for (int j = 0; j < 2; j++) sg2[j] = ld8(rs, WS_STAT2 + (64 * T + 32 * j + (lane & 31)) * 8);
        }
#pragma unroll
        for (int j = 0; j < 2; j++) {
          const u32 sv = sg2[j].x;
          const float mu = bflo(sv), rsd = __builtin_amdgcn_rcpf(bfhi(sv));
          u32 hp[4];
#pragma unroll
          for (int gq = 0; gq < 2; gq++) {
            LAS unsigned char *cl = S + LD_CSL + w * 256 + (8 * gq + 4 * h) * 4;
            const f32x4 csg = *(LAS f32x4 *)(cl), csu = *(LAS f32x4 *)(cl + 64);
            const f32x4 bg = *(LAS f32x4 *)(cl + 128), bu = *(LAS f32x4 *)(cl + 192);
            float hv[4];
#pragma unroll
            for (int i = 0; i < 4; i++) {
              const float gg = rsd * (acc[j][4 * gq + i] - mu * csg[i]) + bg[i];
              const float uu = rsd * (acc[j][8 + 4 * gq + i] - mu * csu[i]) + bu[i];
              hv[i] = gg * uu;
            }
            hp[2 * gq] = pkbf(hv[0], hv[1]);
            hp[2 * gq + 1] = pkbf(hv[2], hv[3]);
          }
          st16(rs, WS_HF + ((2 * T + j) * 112 + phi) * 1024 + lane * 16, v4u{hp[0], hp[1], hp[2], hp[3]});
        }
        if (hf == 0) {
          BAR();
        } else {
          DRAIN();
          BAR();
          if (tid == 0) __hip_atomic_fetch_add((gu32 *)(p.ws + WS_CNT) + (256 + 16 + tt) * 64, 1u, RLX_AGENT);
        }
        STAMP(10 + hf);
      }
    }

    v4u gbg[2];
    {
      const int dq = tid & 15;
      gbg[0] = ld16(rs, WS_MODG + (5 * D + 64 * dtile + 4 * dq) * 8);
      gbg[1] = ld16(rs, WS_MODG + (5 * D + 64 * dtile + 4 * dq + 2) * 8);
    }
#pragma unroll
    for (int hf = 0; hf < 2; hf++) {
      const int tau = 32 * hf + t2;
      if (w == 0) poll_counters(p, 256 + hf * 16 + (t2 >> 1), 1, 14u, lane);
      BAR();
      STAMP(12 + 2 * hf);
      v4u hb[14];
#pragma unroll
      for (int k = 0; k < 14; k++) hb[k] = ld16(rs, WS_HF + (tau * 112 + 14 * w + k) * 1024 + lane * 16);
      v4u res = {0, 0, 0, 0};
      if (tid < 256) res = ld16(rs, WS_X1T + ((tau >> 1) * D + 64 * dtile + (tid >> 2)) * 128 + (tau & 1) * 64 + (tid & 3) * 16);
      PIN();
      f32x16 acc[2];
#pragma unroll
      for (int j = 0; j < 2; j++)
#pragma unroll
        for (int i = 0; i < 16; i++) acc[j][i] = 0.f;
#pragma unroll
      for (int k = 0; k < 14; k++) {
        acc[0] = mfma32b(wd[k], hb[k], acc[0]);
        acc[1] = mfma32b(wd[14 + k], hb[k], acc[1]);
      }
      {
        const int col = lane & 31, h = lane >> 5;
        LAS unsigned char *rd = S + LD_RED + w * RED_W + col * RED_ROW + h * 16;
#pragma unroll
        for (int dl = 0; dl < 2; dl++)
#pragma unroll
          for (int gq = 0; gq < 4; gq++)
            *(LAS f32x4 *)(rd + dl * 128 + gq * 32) =
                f32x4{acc[dl][4 * gq], acc[dl][4 * gq + 1], acc[dl][4 * gq + 2], acc[dl][4 * gq + 3]};
        if (tid < 256) *(LAS v4u *)(S + LD_RES + (tid >> 2) * 64 + (tid & 3) * 16) = res;
      }
      BAR();
      {
        const int tok = tid >> 4, dq = tid & 15;
        f32x4 sum = *(LAS f32x4 *)(S + LD_RED + tok * RED_ROW + dq * 16);
#pragma unroll
        for (int ww = 1; ww < 8; ww++) sum += *(LAS f32x4 *)(S + LD_RED + ww * RED_W + tok * RED_ROW + dq * 16);
        LAS u16 *rp = (LAS u16 *)(S + LD_RES) + (4 * dq) * 32 + tok;
        const float o0 = bf2f(rp[0]) + asf(gbg[0].x) * sum[0];
        const float o1 = bf2f(rp[32]) + asf(gbg[0].z) * sum[1];
        const float o2 = bf2f(rp[64]) + asf(gbg[1].x) * sum[2];
        const float o3 = bf2f(rp[96]) + asf(gbg[1].z) * sum[3];
        const int l = 32 * tau + tok;
        *(GAS v2u *)(p.out + l * D + 64 * dtile + 4 * dq) = v2u{pkbf(o0, o1), pkbf(o2, o3)};
      }
      STAMP(13 + 2 * hf);
      if (hf == 0) BAR();
    }
  } else {
    if (sidx < 28) {
      float a[8] = {0.f, 0.f, 0.f, 0.f, 0.f, 0.f, 0.f, 0.f};
#pragma unroll
      for (int i = 0; i < 11; i++) {
        const float x = bf2f(cv[i]);
        const float sl = rbf(x / (1.0f + expf(-x)));
        a[0] += sl * bflo(wm[i].x); a[1] += sl * bfhi(wm[i].x); a[2] += sl * bflo(wm[i].y); a[3] += sl * bfhi(wm[i].y);
        a[4] += sl * bflo(wm[i].z); a[5] += sl * bfhi(wm[i].z); a[6] += sl * bflo(wm[i].w); a[7] += sl * bfhi(wm[i].w);
      }
      if (tid < 504) {
        LAS float *rp = (LAS float *)(S) + (tid / 12) * 96 + (tid % 12) * 8;
        *(LAS f32x4 *)rp = f32x4{a[0], a[1], a[2], a[3]};
        *(LAS f32x4 *)(rp + 4) = f32x4{a[4], a[5], a[6], a[7]};
      }
    }
    DRAIN();
    BAR();
    if (tid < 8) st8(rs, WS_STAT1 + b * 128 + tid * 8, v2u{*(LAS u32 *)(S + LD_STAT + tid * 4), tag});
    if (sidx < 28 && tid < 96) {
      float s = 0.f;
#pragma unroll
      for (int i = 0; i < 42; i++) s += ((LAS float *)S)[i * 96 + tid];
      st8(rs, WS_MODG + (96 * sidx + tid) * 8, v2u{__builtin_bit_cast(u32, rbf(s)), tag});
    }
    STAMP(3);
    (void)wait_granule(rs, WS_STAT1 + t8 * 128, tag, true);
    {
      const int dd = tid < D ? tid : 0;
      const u32 vcb = wait_granule(rs, WS_MODG + (4 * D + dd) * 8, tag, true);
      const u32 vsb = wait_granule(rs, WS_MODG + (3 * D + dd) * 8, tag, true);
      if (tid < D) {
        ((LAS u16 *)(S + LD_CV))[tid] = (u16)(pkbf(1.0f + __builtin_bit_cast(float, vcb), 0.f) & 0xFFFFu);
        ((LAS u16 *)(S + LD_CV))[D + tid] = (u16)(vsb >> 16);
      }
    }
    BAR();
    STAMP(5);
    if (sidx < 28) {
      const int ph = 4 * sidx + (w >> 1), s0 = 14 * (w & 1);
      v4u wf[14];
#pragma unroll
      for (int k = 0; k < 14; k++) wf[k] = ld16(rs, WS_W1F + (ph * 28 + s0 + k) * 1024 + lane * 16);
      f32x16 acc;
#pragma unroll
      for (int i = 0; i < 16; i++) acc[i] = 0.f;
      LAS unsigned char *cvp = S + LD_CV + ((lane & 31) == 1 ? D * 2 : 0) + (lane >> 5) * 16;
#pragma unroll
      for (int k = 0; k < 14; k++) acc = mfma32b(wf[k], *(LAS v4u *)(cvp + (s0 + k) * 32), acc);
      LAS float *pr = (LAS float *)(S) + w * 64;
      if ((lane & 31) < 2) {
#pragma unroll
        for (int i = 0; i < 16; i++) pr[(lane & 31) * 32 + (i & 3) + 8 * (i >> 2) + 4 * (lane >> 5)] = acc[i];
      }
    }
    BAR();
    if (sidx < 28 && tid < 256) {
      const int pl = tid >> 6, e = tid & 63;
      const float v = ((LAS float *)S)[(2 * pl) * 64 + e] + ((LAS float *)S)[(2 * pl + 1) * 64 + e];
      st8(rs, WS_CSB + ((4 * sidx + pl) * 64 + e) * 8, v2u{__builtin_bit_cast(u32, v), tag});
    }
    STAMP(6);
#pragma unroll 1
    for (int hf = 0; hf < 2; hf++) {
      if (w == 0) poll_counters(p, hf * 128 + g * 16, 16, 14u, lane);
      BAR();
      const int tau = 32 * hf + sidx;
      const int tp = lane & 15, rg = (lane >> 4) + 4 * w;
      u32 xv[14];
#pragma unroll
      for (int i = 0; i < 14; i++)
        xv[i] = __builtin_amdgcn_raw_buffer_load_b32(rs, WS_X1T + ((tau >> 1) * D + rg + 32 * i) * 128 + (tau & 1) * 64 + tp * 4, 0, SC1);
      float s0 = 0.f, s1 = 0.f, q0 = 0.f, q1 = 0.f;
#pragma unroll
      for (int i = 0; i < 14; i++) {
        const float a = bflo(xv[i]), c = bfhi(xv[i]);
        s0 += a; s1 += c; q0 += a * a; q1 += c * c;
      }
      LAS f32x4 *pr = (LAS f32x4 *)(S + 4096);
      pr[rg * 16 + tp] = f32x4{s0, s1, q0, q1};
      BAR();
      if (tid < 32) {
        float s = 0.f, q = 0.f;
#pragma unroll
        for (int i = 0; i < 32; i++) {
          const f32x4 v = pr[i * 16 + (tid >> 1)];
          s += (tid & 1) ? v[1] : v[0];
          q += (tid & 1) ? v[3] : v[2];
        }
        const float mean = s * (1.0f / D);
        const float var = rbf(fmaxf(q * (1.0f / D) - mean * mean, 0.f));
        const float sd = rbf(__builtin_sqrtf(rbf(var + 1e-5f)));
        st8(rs, WS_STAT2 + (32 * tau + tid) * 8, v2u{pkbf(mean, sd), tag});
      }
      STAMP(8 + hf);
    }
  }
  asm volatile("" ::"v"(pf));
  STAMP(16);
}

}

extern "C" void kernel_launch(void *const *d_in, const int *in_sizes, int n_in, void *d_out, int out_size, void *d_ws,
                              size_t ws_size, hipStream_t stream) {
  using namespace mk;
  Params p;
  p.x = (const u16 *)d_in[0];
  p.Are = (const u16 *)d_in[1]; p.Aim = (const u16 *)d_in[2];
  p.Bre = (const u16 *)d_in[3]; p.Bim = (const u16 *)d_in[4];
  p.Cre = (const u16 *)d_in[5]; p.Cim = (const u16 *)d_in[6];
  p.logdt = (const u16 *)d_in[7]; p.Dsk = (const u16 *)d_in[8];
  p.wg = (const u16 *)d_in[10]; p.wu = (const u16 *)d_in[11]; p.wd = (const u16 *)d_in[12];
  p.cvec = (const u16 *)d_in[13]; p.wmod = (const u16 *)d_in[14];
  p.out = (u16 *)d_out;
  p.ws = (unsigned char *)d_ws;
  (void)hipFuncSetAttribute((const void *)k_main, hipFuncAttributeMaxDynamicSharedMemorySize, LDS_BYTES);
  k_main<<<NWG, NTHR, LDS_BYTES, stream>>>(p);
}
```

```cpp
#include <hip/hip_runtime.h>
#include <stdint.h>

namespace mk {

typedef unsigned short u16;
typedef unsigned u32;
typedef u32 v4u __attribute__((ext_vector_type(4)));
typedef u32 v2u __attribute__((ext_vector_type(2)));
typedef float f2 __attribute__((ext_vector_type(2)));
typedef float f32x4 __attribute__((ext_vector_type(4)));
typedef float f32x16 __attribute__((ext_vector_type(16)));
typedef _Float16 h2 __attribute__((ext_vector_type(2)));
typedef _Float16 h8 __attribute__((ext_vector_type(8)));
typedef __bf16 b2 __attribute__((ext_vector_type(2)));
typedef __bf16 b8 __attribute__((ext_vector_type(8)));
typedef short s4 __attribute__((ext_vector_type(4)));
#define LAS __attribute__((address_space(3)))
#define GAS __attribute__((address_space(1)))
typedef GAS u32 gu32;
#define RLX_AGENT __ATOMIC_RELAXED, __HIP_MEMORY_SCOPE_AGENT

constexpr int L = 2048, D = 448, NS = 256, F = 1792;
constexpr int NWG = 256, NTHR = 512;
constexpr int SC1 = 16;
constexpr int PF_LINES = 190;

constexpr int WS_EPOCH = 0;
constexpr int WS_CNT = 4096;
constexpr int NCNT = 288;
constexpr int WS_STAT1 = WS_CNT + NCNT * 256;
constexpr int WS_MODG = WS_STAT1 + 256 * 128;
constexpr int WS_CSB = WS_MODG + 2688 * 8;
constexpr int WS_STAT2 = WS_CSB + 112 * 64 * 8;
constexpr int WS_WRDY = WS_STAT2 + 2048 * 8;
constexpr int WS_XG = WS_WRDY + 224 * 128;
constexpr int WS_W1F = WS_XG + 256 * 2 * 448 * 16;
constexpr int WS_WDF = WS_W1F + 112 * 28 * 1024;
constexpr int WS_X1T = WS_WDF + 14 * 112 * 1024;
constexpr int WS_X1S = WS_X1T + 32 * 448 * 128;
constexpr int WS_HF = WS_X1S + 32 * 448 * 128;
constexpr int WS_DBG = WS_HF + 64 * 112 * 1024;
constexpr int WS_END = WS_DBG + (8 << 20);
static_assert(WS_STAT1 % 128 == 0 && WS_MODG % 128 == 0 && WS_CSB % 128 == 0 && WS_STAT2 % 128 == 0 && WS_XG % 128 == 0 && WS_W1F % 128 == 0, "align");

constexpr int PQ_ROW = 1056, PQ_CH = 57 * PQ_ROW;
constexpr int LD_PQ = 0;
constexpr int LD_KC = 0;
constexpr int KC_CP = 4160, KC_CH = 4 * KC_CP;
constexpr int LD_EX = 33280;
constexpr int LD_XB = 0;
constexpr int XB_SZ = 448 * 128;
constexpr int LD_RED = 0;
constexpr int RED_ROW = 272, RED_W = 32 * RED_ROW;
constexpr int LD_RES = 8 * RED_W;
constexpr int LD_KD = 120384;
constexpr int KD_CH = 784 * 2;
constexpr int LD_U = LD_KD + 2 * KD_CH;
constexpr int U_CH = 2304 * 2;
constexpr int LD_XS = LD_U + 2 * U_CH;
constexpr int LD_XROW = 120384;
constexpr int LD_WSTG = LD_XROW + 8 * 960;
constexpr int WSTG_ROW = 464, WSTG_M = 16 * WSTG_ROW;
constexpr int LD_SMALL = 150400;
constexpr int LD_STAT = LD_SMALL;
constexpr int LD_WMAX = LD_SMALL + 32;
constexpr int LD_MODP = LD_SMALL + 64;
constexpr int LD_CSL = LD_SMALL + 512;
constexpr int LD_CV = LD_CSL + 2048;
constexpr int LD_SIL = LD_CV + 2 * 448 * 2;
constexpr int LD_STAMP = LD_SIL + 448 * 4;
constexpr int LD_END = LD_STAMP + 32 * 4;
constexpr int LDS_BYTES = 156 * 1024;
static_assert(LD_XS + 8192 <= LD_SMALL && LD_WSTG + 3 * WSTG_M <= LD_SMALL && LD_END <= LDS_BYTES, "lds");
static_assert(LD_RES + 4096 <= LD_KD, "lds red");

struct Params {
  const u16 *x, *Are, *Aim, *Bre, *Bim, *Cre, *Cim, *logdt, *Dsk, *wg, *wu, *wd, *cvec, *wmod;
  u16 *out;
  unsigned char *ws;
};

struct KTab { u16 v[256][16]; };
constexpr int bf16_round_int(int j) {
  if (j < 256) return j;
  int sh = 0; int t = j; while (t >= 256) { t >>= 1; sh++; }
  int q = j >> sh, rem = j & ((1 << sh) - 1), half = 1 << (sh - 1);
  if (rem > half || (rem == half && (q & 1))) q++;
  return q << sh;
}
constexpr int kd_index(int j) {
  if (j < 0) return 768;
  int l = bf16_round_int(j);
  if (l >= 2048) return 40 * 16;
  int k = l < 256 ? 0 : (l < 512 ? 1 : (l < 1024 ? 2 : 3));
  int base = k ? (128 << k) : 0;
  int rem = l - base;
  int a = rem >> 7, b = (rem >> 3) & 15, c = (rem & 7) >> k;
  int row = (k ? 8 + 8 * k : 0) + a * (8 >> k) + c;
  return row * 16 + b;
}
constexpr KTab make_ktab() {
  KTab t{};
  for (int i = 0; i < 256; i++)
    for (int j = 0; j < 16; j++) t.v[i][j] = (u16)(j < 11 ? kd_index(8 * i - 3 + j) : 768);
  return t;
}
__device__ const KTab g_ktab = make_ktab();

__device__ __forceinline__ float asf(u32 v) { return __builtin_bit_cast(float, v); }
__device__ __forceinline__ h2 ash2(u32 v) { return __builtin_bit_cast(h2, v); }
__device__ __forceinline__ float bf2f(u32 v) { return __builtin_bit_cast(float, v << 16); }
__device__ __forceinline__ float bflo(u32 v) { return __builtin_bit_cast(float, v << 16); }
__device__ __forceinline__ float bfhi(u32 v) { return __builtin_bit_cast(float, v & 0xFFFF0000u); }
__device__ __forceinline__ u32 pkbf(float a, float b) {
  return __builtin_bit_cast(u32, __builtin_convertvector(f2{a, b}, b2));
}
__device__ __forceinline__ u32 pkh(float a, float b) {
  return __builtin_bit_cast(u32, __builtin_convertvector(f2{a, b}, h2));
}
__device__ __forceinline__ float rbf(float a) { return bflo(pkbf(a, a)); }
__device__ __forceinline__ f2 cmul(f2 a, f2 b) {
  f2 t = f2{a.x, a.x} * b;
  return __builtin_elementwise_fma(f2{a.y, a.y}, f2{-b.y, b.x}, t);
}
template <int CTRL, int RM> __device__ __forceinline__ float dpp0(float v) {
  return __builtin_bit_cast(float, __builtin_amdgcn_update_dpp(0, __builtin_bit_cast(int, v), CTRL, RM, 0xF, true));
}
__device__ __forceinline__ float wave_sum(float v) {
  v += dpp0<0x111, 0xF>(v);
  v += dpp0<0x112, 0xF>(v);
  v += dpp0<0x114, 0xF>(v);
  v += dpp0<0x118, 0xF>(v);
  v += dpp0<0x142, 0xA>(v);
  v += dpp0<0x143, 0xC>(v);
  return __builtin_bit_cast(float, __builtin_amdgcn_readlane(__builtin_bit_cast(int, v), 63));
}
__device__ __forceinline__ float wave_max_nonneg(float v) {
  v = fmaxf(v, dpp0<0x111, 0xF>(v));
  v = fmaxf(v, dpp0<0x112, 0xF>(v));
  v = fmaxf(v, dpp0<0x114, 0xF>(v));
  v = fmaxf(v, dpp0<0x118, 0xF>(v));
  v = fmaxf(v, dpp0<0x142, 0xA>(v));
  v = fmaxf(v, dpp0<0x143, 0xC>(v));
  return __builtin_bit_cast(float, __builtin_amdgcn_readlane(__builtin_bit_cast(int, v), 63));
}
__device__ __forceinline__ v2u trread(LAS unsigned char *p) {
  return __builtin_bit_cast(v2u, __builtin_amdgcn_ds_read_tr16_b64_v4i16((LAS s4 *)p));
}
__device__ __forceinline__ v4u tr8(LAS unsigned char *p0, LAS unsigned char *p1) {
  v2u a = trread(p0), b = trread(p1);
  return v4u{a.x, a.y, b.x, b.y};
}
__device__ __forceinline__ f32x4 mfma16h(v4u a, v4u b, f32x4 c) {
  return __builtin_amdgcn_mfma_f32_16x16x32_f16(__builtin_bit_cast(h8, a), __builtin_bit_cast(h8, b), c, 0, 0, 0);
}
__device__ __forceinline__ f32x16 mfma32b(v4u a, v4u b, f32x16 c) {
  return __builtin_amdgcn_mfma_f32_32x32x16_bf16(__builtin_bit_cast(b8, a), __builtin_bit_cast(b8, b), c, 0, 0, 0);
}
#define BAR() asm volatile("s_waitcnt lgkmcnt(0)\n\ts_barrier" ::: "memory")
#define DRAIN() asm volatile("s_waitcnt vmcnt(0)" ::: "memory")
#define PIN() do { asm volatile("" ::: "memory"); __builtin_amdgcn_sched_barrier(0); } while (0)
typedef __amdgpu_buffer_rsrc_t rsrc_t;
__device__ __forceinline__ v4u ld16(rsrc_t rs, int off) { return __builtin_amdgcn_raw_buffer_load_b128(rs, off, 0, SC1); }
__device__ __forceinline__ v4u ld16s(rsrc_t rs, int voff, int soff) { return __builtin_amdgcn_raw_buffer_load_b128(rs, voff, soff, SC1); }
__device__ __forceinline__ void st16s(rsrc_t rs, int voff, int soff, v4u v) { __builtin_amdgcn_raw_buffer_store_b128(v, rs, voff + soff, 0, SC1); }
__device__ __forceinline__ v2u ld8(rsrc_t rs, int off) { return __builtin_amdgcn_raw_buffer_load_b64(rs, off, 0, SC1); }
__device__ __forceinline__ void st16(rsrc_t rs, int off, v4u v) { __builtin_amdgcn_raw_buffer_store_b128(v, rs, off, 0, SC1); }
__device__ __forceinline__ void st8(rsrc_t rs, int off, v2u v) { __builtin_amdgcn_raw_buffer_store_b64(v, rs, off, 0, SC1); }
__device__ __forceinline__ void st4(rsrc_t rs, int off, u32 v) { __builtin_amdgcn_raw_buffer_store_b32(v, rs, off, 0, SC1); }

constexpr unsigned SPIN_CAP = 1u << 14;

#define STAMP(i)

__device__ __forceinline__ void poll_counters(const Params &p, int idx0, int ns, u32 want, int lane) {
  gu32 *cw = (gu32 *)(p.ws + WS_CNT) + (idx0 + (lane < ns ? lane : 0)) * 64;
#pragma unroll 1
  for (unsigned sp = 0; sp < SPIN_CAP; sp++) {
    const u32 v = __hip_atomic_load(cw, RLX_AGENT);
    if (__all(v >= want)) break;
    __builtin_amdgcn_s_sleep(1);
  }
  __builtin_amdgcn_fence(__ATOMIC_ACQUIRE, "wavefront");
}
__device__ __forceinline__ u32 finish_granule(rsrc_t rs, int off, u32 tag, v2u g) {
#pragma unroll 1
  for (unsigned sp = 0; sp < SPIN_CAP; sp++) {
    if (__all(g.y == tag)) break;
    __builtin_amdgcn_s_sleep(1);
    g = ld8(rs, off);
  }
  __builtin_amdgcn_fence(__ATOMIC_ACQUIRE, "wavefront");
  return g.x;
}

template <int ROW> __device__ __forceinline__ void pqw4(u32 m0v, f2 a, f2 b, f2 c, f2 d) {
  const u32 h0 = pkh(a.x, a.y), h1 = pkh(b.x, b.y), h2_ = pkh(c.x, c.y), h3 = pkh(d.x, d.y);
  asm volatile("s_mov_b32 m0, %4\n\ts_nop 0\n\tds_write_addtid_b32 %0 offset:%c5\n\tds_write_addtid_b32 %1 offset:%c6\n\t"
               "ds_write_addtid_b32 %2 offset:%c7\n\tds_write_addtid_b32 %3 offset:%c8"
               ::"v"(h0), "v"(h1), "v"(h2_), "v"(h3), "s"(m0v), "i"(ROW * PQ_ROW), "i"((ROW + 1) * PQ_ROW),
               "i"((ROW + 2) * PQ_ROW), "i"((ROW + 3) * PQ_ROW) : "memory");
}
template <int ROW> __device__ __forceinline__ void pqw1(u32 m0v, f2 a) {
  const u32 h0 = pkh(a.x, a.y);
  asm volatile("s_mov_b32 m0, %1\n\ts_nop 0\n\tds_write_addtid_b32 %0 offset:%c2" ::"v"(h0), "s"(m0v), "i"(ROW * PQ_ROW) : "memory");
}
__device__ __forceinline__ f2 zpow(float al, float rv) {
  const float e = __builtin_amdgcn_exp2f(al);
  const float r = __builtin_amdgcn_fractf(rv);
  return f2{e * __builtin_amdgcn_cosf(r), e * __builtin_amdgcn_sinf(r)};
}
__device__ __forceinline__ float pow2b(int e) { return asf((u32)e << 23); }

__device__ __forceinline__ void ln1_row(LAS unsigned char *S, v4u xr, int w, int lane) {
  float xv[8] = {bflo(xr.x), bfhi(xr.x), bflo(xr.y), bfhi(xr.y), bflo(xr.z), bfhi(xr.z), bflo(xr.w), bfhi(xr.w)};
  float s = ((xv[0] + xv[1]) + (xv[2] + xv[3])) + ((xv[4] + xv[5]) + (xv[6] + xv[7]));
  s = lane < 56 ? s : 0.f;
  const float mean = wave_sum(s) * (1.0f / D);
  float q = 0.f;
#pragma unroll
  for (int i = 0; i < 8; i++) { const float e = xv[i] - mean; q += e * e; }
  q = lane < 56 ? q : 0.f;
  const float var = rbf(wave_sum(q) * (1.0f / D));
  const float sd = rbf(__builtin_amdgcn_sqrtf(rbf(var + 1e-5f)));
  if (lane == 0) *(LAS u32 *)(S + LD_STAT + w * 4) = pkbf(mean, sd);
  if (lane < 56) *(LAS v4u *)(S + LD_XROW + w * 960 + lane * 16) = xr;
}

__device__ __forceinline__ void publish_xt(LAS unsigned char *S, const rsrc_t rs, int lane, int w, int tid, int b, u32 tag) {
  const int G = lane >> 4, e = (lane >> 2) & 3, pp = lane & 3;
  if (w < 7) {
    LAS unsigned char *ax = S + LD_XROW + e * 960 + (64 * w + 16 * G + 4 * pp) * 2;
    const v4u vx = tr8(ax, ax + 4 * 960);
    st16s(rs, lane * 16, WS_XG + ((b * 2 + 0) * D + 64 * w) * 16, v4u{vx.x, tag, vx.y, tag});
    st16s(rs, lane * 16, WS_XG + ((b * 2 + 1) * D + 64 * w) * 16, v4u{vx.z, tag, vx.w, tag});
  } else if (lane < 8) {
    st8(rs, WS_STAT1 + b * 128 + lane * 8, v2u{*(LAS u32 *)(S + LD_STAT + lane * 4), tag});
  }
}

__device__ __forceinline__ void main_role(const Params &p, LAS unsigned char *S, const rsrc_t rs, const int tid,
                                          const int lane, const int w, const int b, const u32 epoch, v4u xr) {
  const int g = b & 7, r = b >> 3, m = 28 * g + r;
  const int cch = w >> 2, t8 = tid & 255, dch = 2 * m + cch;
  const int lane16 = lane * 16;
  const int tc = tid < 448 ? tid : 447;
  v2u wm[3];
#pragma unroll
  for (int i = 0; i < 3; i++) wm[i] = *(const GAS v2u *)(p.wmod + tc * (6 * D) + 12 * m + 4 * i);
  const u32 cvv = p.cvec[tc];
  const int po = dch * NS + t8;
  const u32 pAre = p.Are[po], pAim = p.Aim[po], pBre = p.Bre[po], pBim = p.Bim[po], pCre = p.Cre[po], pCim = p.Cim[po];
  const u32 pLdt = p.logdt[dch], pDsk = p.Dsk[dch];
  PIN();
  const int srow = tc / 28, scol = tc % 28;
  const v4u ws0 = *(const GAS v4u *)(p.wg + (16 * (m >> 3) + srow) * F + 224 * (m & 7) + 8 * scol);
  const v4u ws1 = *(const GAS v4u *)(p.wu + (16 * (m >> 3) + srow) * F + 224 * (m & 7) + 8 * scol);
  const v4u ws2 = *(const GAS v4u *)(p.wd + (16 * (m >> 1) + srow) * D + 224 * (m & 1) + 8 * scol);
  const v4u kt0 = *(const GAS v4u *)(&g_ktab.v[t8][0]);
  const v4u kt1 = *(const GAS v4u *)(&g_ktab.v[t8][8]);
  PIN();
  asm volatile("" : "+v"(xr));
  const u32 tag = __builtin_amdgcn_readfirstlane(epoch) + 1u;

  ln1_row(S, xr, w, lane);
  {
    const float cx = bf2f(cvv);
    float sl = rbf(cx * __builtin_amdgcn_rcpf(1.0f + __expf(-cx)));
    sl = tid < 448 ? sl : 0.f;
    float pr[12];
#pragma unroll
    for (int i = 0; i < 3; i++) {
      pr[4 * i + 0] = sl * bflo(wm[i].x); pr[4 * i + 1] = sl * bfhi(wm[i].x);
      pr[4 * i + 2] = sl * bflo(wm[i].y); pr[4 * i + 3] = sl * bfhi(wm[i].y);
    }
#pragma unroll
    for (int i = 0; i < 12; i++) {
      float v = pr[i];
      v += dpp0<0x111, 0xF>(v); v += dpp0<0x112, 0xF>(v); v += dpp0<0x114, 0xF>(v); v += dpp0<0x118, 0xF>(v);
      v += dpp0<0x142, 0xA>(v); v += dpp0<0x143, 0xC>(v);
      pr[i] = v;
    }
    if (lane == 63) {
      LAS f32x4 *mp = (LAS f32x4 *)(S + LD_MODP + w * 48);
      mp[0] = f32x4{pr[0], pr[1], pr[2], pr[3]}; mp[1] = f32x4{pr[4], pr[5], pr[6], pr[7]}; mp[2] = f32x4{pr[8], pr[9], pr[10], pr[11]};
    }
  }
  STAMP(1);
  BAR();
  publish_xt(S, rs, lane, w, tid, b, tag);
  if (w == 7 && lane >= 16 && lane < 28) {
    float sm = 0.f;
#pragma unroll
    for (int i = 0; i < 7; i++) sm += ((LAS float *)(S + LD_MODP))[i * 12 + lane - 16];
    st8(rs, WS_MODG + (12 * m + lane - 16) * 8, v2u{__builtin_bit_cast(u32, rbf(sm)), tag});
  }
  PIN();
  STAMP(2);

  const float dt = rbf(__expf(bf2f(pLdt)));
  const float al = dt * bf2f(pAre) * 1.44269504088896f;
  const float rv = dt * bf2f(pAim) * 0.159154943091895f;
  const f2 Wc = cmul(f2{bf2f(pCre), bf2f(pCim)}, f2{dt * bf2f(pBre), dt * bf2f(pBim)});
  {
    const float mx = wave_max_nonneg(fmaxf(fabsf(Wc.x), fabsf(Wc.y)));
    if (lane == 0) *(LAS float *)(S + LD_WMAX + w * 4) = mx;
  }
  const u32 m0v = (u32)(LD_PQ + (w >> 2) * PQ_CH + (w & 3) * 256);
  const f2 p1 = zpow(al, rv), z8 = zpow(al * 8.f, rv * 8.f), Z1 = zpow(al * 128.f, rv * 128.f);
  const f2 p2 = cmul(p1, p1), p3 = cmul(p2, p1), p4 = cmul(p2, p2), p5 = cmul(p4, p1), p6 = cmul(p4, p2), p7 = cmul(p4, p3);
  pqw4<0>(m0v, f2{1.f, 0.f}, p1, p2, p3);
  pqw4<4>(m0v, p4, p5, p6, p7);
  pqw4<8>(m0v, Z1, cmul(Z1, p1), cmul(Z1, p2), cmul(Z1, p3));
  pqw4<12>(m0v, cmul(Z1, p4), cmul(Z1, p5), cmul(Z1, p6), cmul(Z1, p7));
  if (tid < 448) {
    *(LAS v4u *)(S + LD_WSTG + 0 * WSTG_M + srow * WSTG_ROW + scol * 16) = ws0;
    *(LAS v4u *)(S + LD_WSTG + 1 * WSTG_M + srow * WSTG_ROW + scol * 16) = ws1;
    *(LAS v4u *)(S + LD_WSTG + 2 * WSTG_M + srow * WSTG_ROW + scol * 16) = ws2;
  }
  BAR();
  {
    const int G = lane >> 4, e = (lane >> 2) & 3, pp = lane & 3, h = G >> 1;
    const int w7 = w < 7 ? w : 6, j1 = w + 8 < 14 ? w + 8 : 13;
    LAS unsigned char *a0 = S + LD_WSTG + (G & 1) * WSTG_M + (8 * h + e) * WSTG_ROW + (16 * w + 4 * pp) * 2;
    LAS unsigned char *a1 = S + LD_WSTG + (G & 1) * WSTG_M + (8 * h + e) * WSTG_ROW + (16 * j1 + 4 * pp) * 2;
    LAS unsigned char *a2 = S + LD_WSTG + 2 * WSTG_M + (4 * h + e) * WSTG_ROW + (32 * w7 + 16 * (G & 1) + 4 * pp) * 2;
    const v4u v0 = tr8(a0, a0 + 4 * WSTG_ROW), v1 = tr8(a1, a1 + 4 * WSTG_ROW), v2 = tr8(a2, a2 + 8 * WSTG_ROW);
    st16s(rs, lane16, WS_W1F + ((14 * (m & 7) + w) * 28 + (m >> 3)) * 1024, v0);
    if (w < 6) st16s(rs, lane16, WS_W1F + ((14 * (m & 7) + w + 8) * 28 + (m >> 3)) * 1024, v1);
    if (w < 7) st16s(rs, lane16, WS_WDF + ((7 * (m & 1) + w) * 112 + (m >> 1)) * 1024, v2);
    if (w == 7 && lane < 2 && m + 224 * lane < NCNT) st4(rs, WS_CNT + (m + 224 * lane) * 256, 0u);
  }
  PIN();
  STAMP(3);
  int ebW;
  {
    const f32x4 mx4 = *(LAS f32x4 *)(S + LD_WMAX + (w & 4) * 4);
    const float mx = fmaxf(fmaxf(mx4[0], mx4[1]), fmaxf(mx4[2], mx4[3]));
    ebW = (int)((__builtin_bit_cast(u32, mx) >> 23) & 0xFF);
    ebW = ebW < 14 ? 14 : (ebW > 253 ? 253 : ebW);
  }
  const float sQ = pow2b(267 - ebW);
  {
    const f2 Z2 = cmul(Z1, Z1), Z3 = cmul(Z2, Z1), Z4 = cmul(Z2, Z2);
    pqw4<16>(m0v, Z2, cmul(Z2, p2), cmul(Z2, p4), cmul(Z2, p6));
    pqw4<20>(m0v, Z3, cmul(Z3, p2), cmul(Z3, p4), cmul(Z3, p6));
    const f2 Z5 = cmul(Z4, Z1), Z6 = cmul(Z4, Z2), Z7 = cmul(Z4, Z3), Z8 = cmul(Z4, Z4);
    pqw4<24>(m0v, Z4, cmul(Z4, p4), Z5, cmul(Z5, p4));
    pqw4<28>(m0v, Z6, cmul(Z6, p4), Z7, cmul(Z7, p4));
    pqw4<32>(m0v, Z8, cmul(Z8, Z1), cmul(Z8, Z2), cmul(Z8, Z3));
    pqw4<36>(m0v, cmul(Z8, Z4), cmul(Z8, Z5), cmul(Z8, Z6), cmul(Z8, Z7));
    pqw1<40>(m0v, cmul(Z8, Z8));
    const f2 z16 = cmul(z8, z8), z24 = cmul(z16, z8), z32 = cmul(z16, z16);
    f2 q0 = Wc * sQ, q1 = cmul(q0, z8), q2 = cmul(q0, z16), q3 = cmul(q0, z24);
#define NEGI(q) f2{(q).x, -(q).y}
    pqw4<41>(m0v, NEGI(q0), NEGI(q1), NEGI(q2), NEGI(q3));
    q0 = cmul(q0, z32); q1 = cmul(q1, z32); q2 = cmul(q2, z32); q3 = cmul(q3, z32);
    pqw4<45>(m0v, NEGI(q0), NEGI(q1), NEGI(q2), NEGI(q3));
    q0 = cmul(q0, z32); q1 = cmul(q1, z32); q2 = cmul(q2, z32); q3 = cmul(q3, z32);
    pqw4<49>(m0v, NEGI(q0), NEGI(q1), NEGI(q2), NEGI(q3));
    q0 = cmul(q0, z32); q1 = cmul(q1, z32); q2 = cmul(q2, z32); q3 = cmul(q3, z32);
    pqw4<53>(m0v, NEGI(q0), NEGI(q1), NEGI(q2), NEGI(q3));
#undef NEGI
  }
  v4u sg[4], xg[2];
  v2u mg[4];
  const int so = WS_STAT1 + t8 * 128, mo = WS_MODG + dch * 8, xo = WS_XG + (t8 * 2 * D + dch) * 16;
#define LD_HANDOFF_A() do { \
    _Pragma("unroll") for (int i = 0; i < 4; i++) sg[i] = ld16(rs, so + i * 16); \
    xg[0] = ld16(rs, xo); xg[1] = ld16(rs, xo + D * 16); \
    mg[0] = ld8(rs, mo + 0 * D * 8); mg[1] = ld8(rs, mo + 1 * D * 8); \
    mg[2] = ld8(rs, mo + 2 * D * 8); mg[3] = ld8(rs, mo + 4 * D * 8); } while (0)
  LD_HANDOFF_A();
  PIN();
  STAMP(4);
  asm volatile("s_waitcnt vmcnt(10) ; PUBWAIT" ::: "memory");
  BAR();
  if (tid == 0) st4(rs, WS_WRDY + m * 128, tag);
  {
    const int tile = w & 3;
    if (tile < 3) {
      LAS unsigned char *pa = S + LD_PQ + cch * PQ_CH + (16 * tile + (lane & 15)) * PQ_ROW + (lane >> 4) * 16;
      LAS unsigned char *pb = S + LD_PQ + cch * PQ_CH + (41 + (lane & 15)) * PQ_ROW + (lane >> 4) * 16;
      f32x4 acc0 = {0.f, 0.f, 0.f, 0.f}, acc1 = acc0;
      v4u fa[16], fb[16];
#pragma unroll
      for (int ks = 0; ks < 16; ks++) { fa[ks] = *(LAS v4u *)(pa + ks * 64); fb[ks] = *(LAS v4u *)(pb + ks * 64); }
      PIN();
#pragma unroll
      for (int ks = 0; ks < 16; ks += 2) { acc0 = mfma16h(fa[ks], fb[ks], acc0); acc1 = mfma16h(fa[ks + 1], fb[ks + 1], acc1); }
      acc0 += acc1;
      LAS u16 *kd = (LAS u16 *)(S + LD_KD + cch * KD_CH) + (16 * tile + 4 * (lane >> 4)) * 16 + (lane & 15);
#pragma unroll
      for (int i = 0; i < 4; i++) kd[i * 16] = (u16)(pkh(acc0[i] * (1.0f / 256.0f), 0.f) & 0xFFFFu);
    } else if (lane < 16) {
      ((LAS u16 *)(S + LD_KD + cch * KD_CH))[768 + lane] = 0;
    }
  }
#pragma unroll 1
  for (unsigned sp = 0; sp < SPIN_CAP; sp++) {
    bool ok = xg[0].y == tag && xg[0].w == tag && xg[1].y == tag && xg[1].w == tag;
#pragma unroll
    for (int i = 0; i < 4; i++) ok = ok && sg[i].y == tag && sg[i].w == tag && mg[i].y == tag;
    if (__all(ok)) break;
    __builtin_amdgcn_s_sleep(1);
    LD_HANDOFF_A();
  }
#undef LD_HANDOFF_A
  __builtin_amdgcn_fence(__ATOMIC_ACQUIRE, "wavefront");
  const v4u xt = {xg[0].x, xg[0].z, xg[1].x, xg[1].z};
  STAMP(5);
  BAR();
  if (b == 0 && tid == 0) st4(rs, WS_EPOCH, tag);

  v4u w1[28];
  const int fc = 7 * (g & 1) + r % 7, tt = 4 * (g >> 1) + r / 7;
  const int phi = 8 * fc + w;
  const int w1o = WS_W1F + phi * 28 * 1024;
#define LDW1(s) do { w1[s] = ld16s(rs, lane16, w1o + (s) * 1024); PIN(); } while (0)

  {
    LAS u16 *kd = (LAS u16 *)(S + LD_KD + cch * KD_CH);
    u32 v[11];
    const u32 ix[6] = {kt0.x, kt0.y, kt0.z, kt0.w, kt1.x, kt1.y};
#pragma unroll
    for (int i = 0; i < 11; i++) v[i] = kd[(i & 1) ? (ix[i >> 1] >> 16) : (ix[i >> 1] & 0xFFFFu)];
    LAS unsigned char *kc = S + LD_KC + cch * KC_CH + (16 + 8 * t8) * 2;
#pragma unroll
    for (int e = 0; e < 4; e++) {
      v4u o;
      o.x = v[3 - e] | (v[4 - e] << 16);
      o.y = v[5 - e] | (v[6 - e] << 16);
      o.z = v[7 - e] | (v[8 - e] << 16);
      o.w = v[9 - e] | (v[10 - e] << 16);
      *(LAS v4u *)(kc + e * KC_CP) = o;
    }
    if (t8 < 8) *(LAS v4u *)(S + LD_KC + cch * KC_CH + (t8 >> 1) * KC_CP + (t8 & 1) * 16) = v4u{0, 0, 0, 0};
  }
  const float sa = asf(mg[0].x), ca = asf(mg[1].x), ga = asf(mg[2].x), cb = asf(mg[3].x);
  const float c1 = rbf(1.0f + ca), c2 = rbf(1.0f + cb);
  int ebH = (int)((__builtin_bit_cast(u32, 21.5f * fabsf(c1) + fabsf(sa)) >> 23) & 0xFF);
  ebH = ebH < 14 ? 14 : (ebH > 253 ? 253 : ebH);
  const float sH = pow2b(267 - ebH);
  {
    const u32 xq[4] = {xt.x, xt.y, xt.z, xt.w};
    u32 uo[4];
    const float c1s = c1 * sH, sas = sa * sH;
#pragma unroll
    for (int i = 0; i < 4; i++) {
      const u32 s0 = sg[i].x, s1 = sg[i].z;
      const float h0 = ((bflo(xq[i]) - bflo(s0)) * __builtin_amdgcn_rcpf(bfhi(s0))) * c1s + sas;
      const float h1 = ((bfhi(xq[i]) - bflo(s1)) * __builtin_amdgcn_rcpf(bfhi(s1))) * c1s + sas;
      uo[i] = pkh(h0, h1);
    }
    *(LAS v4u *)(S + LD_U + cch * U_CH + (256 + 8 * t8) * 2) = v4u{uo[0], uo[1], uo[2], uo[3]};
    *(LAS v4u *)(S + LD_XS + cch * 4096 + 16 * t8) = xt;
    if (t8 < 32) *(LAS v4u *)(S + LD_U + cch * U_CH + 16 * t8) = v4u{0, 0, 0, 0};
  }
  const float iH = pow2b(ebH - 13);
  const float A1 = ga * iH * pow2b(ebW - 5);
  const float A2 = ga * bf2f(pDsk) * iH;
  const int fo = WS_WRDY + (tid < 224 ? tid : 0) * 128;
  u32 wr = __builtin_amdgcn_raw_buffer_load_b32(rs, fo, 0, SC1);
  STAMP(6);
  BAR();

  {
    const int c = cch, pq = w & 3;
    const int a15 = lane & 15, q = lane >> 4, e = (lane >> 2) & 3, pp = lane & 3;
    LAS unsigned char *ka = S + LD_KC + c * KC_CH + e * KC_CP + (16 + 16 * (q >> 1) - 8 * (q & 1) + 4 * pp) * 2 + pq * 128;
    LAS unsigned char *ub = S + LD_U + c * U_CH + 512 + 32 * a15 - 32 * (q >> 1) + 16 * (q & 1) - pq * 128;
#define RD_A(i) tr8(ka + (i) * 512 + pi * 64, ka + (i) * 512 + pi * 64 - 8)
#define RD_B(i) (*(LAS v4u *)(ub + (i) * 512 - pi * 64))
#pragma unroll
    for (int hf = 0; hf < 2; hf++) {
      LAS unsigned char *ex = S + LD_EX + hf * 32768 + c * 16384;
      f32x4 acc[4];
#pragma unroll
      for (int i = 0; i < 4; i++) acc[i] = f32x4{0.f, 0.f, 0.f, 0.f};
#pragma unroll
      for (int pi = 0; pi < 2; pi++) {
        v4u A[8], B[8];
        if (hf == 0) {
          A[0] = RD_A(0); B[0] = RD_B(0); B[1] = RD_B(1); B[2] = RD_B(2); B[3] = RD_B(3);
          A[1] = RD_A(1); A[2] = RD_A(2); A[3] = RD_A(3);
        } else {
          A[0] = RD_A(0); B[4] = RD_B(4); B[5] = RD_B(5); B[6] = RD_B(6); B[7] = RD_B(7);
          A[1] = RD_A(1); B[3] = RD_B(3); A[2] = RD_A(2); B[2] = RD_B(2); A[3] = RD_A(3); B[1] = RD_B(1);
          A[4] = RD_A(4); B[0] = RD_B(0); A[5] = RD_A(5); A[6] = RD_A(6); A[7] = RD_A(7);
        }
        PIN();
#pragma unroll
        for (int qq = 0; qq < 8; qq++) {
#pragma unroll
          for (int tl = 0; tl < 4; tl++)
            if (qq <= 4 * hf + tl) acc[tl] = mfma16h(A[qq], B[4 * hf + tl - qq], acc[tl]);
          PIN();
          if (hf == 1 && qq < 7) LDW1(7 * pi + qq);
        }
      }
      STAMP(17 + 2 * hf);
#pragma unroll
      for (int tl = 0; tl < 4; tl++) *(LAS f32x4 *)(ex + (pq * 4 + tl) * 1024 + lane16) = acc[tl];
      if (hf == 0) {
#pragma unroll 1
        for (unsigned sp = 0; sp < SPIN_CAP; sp++) {
          if (__all(wr == tag)) break;
          __builtin_amdgcn_s_sleep(1);
          wr = __builtin_amdgcn_raw_buffer_load_b32(rs, fo, 0, SC1);
        }
        __builtin_amdgcn_fence(__ATOMIC_ACQUIRE, "wavefront");
      } else {
        asm volatile("s_waitcnt vmcnt(14) ; PUBWAIT" ::: "memory");
      }
      BAR();
      if (hf == 1 && tid < 8) __hip_atomic_fetch_add((gu32 *)(p.ws + WS_CNT) + (0 * 128 + tid * 16 + (m & 15)) * 64, 1u, RLX_AGENT);
      const int t = 4 * hf + pq;
      const int l0 = 256 * t + 16 * a15 + 4 * q;
      const f32x4 y0 = *(LAS f32x4 *)(ex + (0 * 4 + pq) * 1024 + lane16), y1 = *(LAS f32x4 *)(ex + (1 * 4 + pq) * 1024 + lane16);
      const f32x4 y2 = *(LAS f32x4 *)(ex + (2 * 4 + pq) * 1024 + lane16), y3 = *(LAS f32x4 *)(ex + (3 * 4 + pq) * 1024 + lane16);
      const v2u hh = *(LAS v2u *)(S + LD_U + c * U_CH + (256 + l0) * 2);
      const v2u xx = *(LAS v2u *)(S + LD_XS + c * 4096 + l0 * 2);
      const f32x4 y = (y0 + y1) + (y2 + y3);
      const h2 ha = ash2(hh.x), hb = ash2(hh.y);
      const float o0 = bflo(xx.x) + A1 * y[0] + A2 * (float)ha.x;
      const float o1 = bfhi(xx.x) + A1 * y[1] + A2 * (float)ha.y;
      const float o2 = bflo(xx.y) + A1 * y[2] + A2 * (float)hb.x;
      const float o3 = bfhi(xx.y) + A1 * y[3] + A2 * (float)hb.y;
      const u32 X0 = pkbf(o0, o1), X1 = pkbf(o2, o3);
      const u32 Y0 = pkbf(bflo(X0) * c2, bfhi(X0) * c2), Y1 = pkbf(bflo(X1) * c2, bfhi(X1) * c2);
      const auto r0 = __builtin_amdgcn_permlane16_swap(X0, Y0, false, false);
      const auto r1 = __builtin_amdgcn_permlane16_swap(X1, Y1, false, false);
      const int l8 = 256 * t + 16 * a15 + 8 * (q >> 1);
      st16(rs, ((q & 1) ? WS_X1S : WS_X1T) + ((l8 >> 6) * D + dch) * 128 + (l8 & 63) * 2, v4u{r0[0], r1[0], r0[1], r1[1]});
      PIN();
      STAMP(18 + 4 * hf);
    }
#pragma unroll
    for (int i = 14; i < 28; i++) LDW1(i);
    asm volatile("s_waitcnt vmcnt(14) ; PUBWAIT" ::: "memory");
    BAR();
    if (tid < 8) __hip_atomic_fetch_add((gu32 *)(p.ws + WS_CNT) + (1 * 128 + tid * 16 + (m & 15)) * 64, 1u, RLX_AGENT);
#undef RD_A
#undef RD_B
  }
  STAMP(7);

  if (w == 0) poll_counters(p, 0 * 128 + g * 16, 16, 14u, lane);
  BAR();
  STAMP(8);
  v4u xs[7];
  const int xso = tid * 16;
  const int xlo = (tid >> 3) * 128 + (((tid & 7) ^ (((tid >> 4) & 1) << 2)) * 16);
#pragma unroll
  for (int i = 0; i < 7; i++) xs[i] = ld16s(rs, xso, WS_X1S + tt * XB_SZ + i * 8192);
  PIN();
#pragma unroll
  for (int i = 0; i < 7; i++) *(LAS v4u *)(S + LD_XB + xlo + i * 8192) = xs[i];
  BAR();
  STAMP(9);
  v4u wd[28];
  v2u sg2[2], csg_ = {0, 0};
  u32 warm = 0;
  const int dtile = r % 7, t2 = 4 * g + r / 7;
  const int wdo = WS_WDF + (2 * dtile * 112 + 14 * w) * 1024;
  const int cso = WS_CSB + (phi * 64 + lane) * 8;
  {
    const int G = lane >> 4, e = (lane >> 2) & 3, pp = lane & 3, h = lane >> 5;
    const int sw = ((e >> 1) & 1) * 64;
    LAS unsigned char *bx0 = S + LD_XB + (8 * h + e) * 128 + ((0 + 32 * (G & 1) + 8 * pp) ^ sw);
    LAS unsigned char *bx1 = S + LD_XB + (8 * h + e) * 128 + ((64 + 32 * (G & 1) + 8 * pp) ^ sw);
#pragma unroll
    for (int hf = 0; hf < 2; hf++) {
      f32x16 acc[2];
#pragma unroll
      for (int j = 0; j < 2; j++)
#pragma unroll
        for (int i = 0; i < 16; i++) acc[j][i] = 0.f;
      v4u fb0[3], fb1[3];
      LAS unsigned char *bh0 = bx0 + hf * XB_SZ, *bh1 = bx1 + hf * XB_SZ;
      asm volatile("" : "+v"(bh0), "+v"(bh1));
#define RD_X(s) do { const int o_ = (s) * 2048; fb0[(s) % 3] = tr8(bh0 + o_, bh0 + o_ + 512); fb1[(s) % 3] = tr8(bh1 + o_, bh1 + o_ + 512); } while (0)
      RD_X(0); RD_X(1);
      const int sto = WS_STAT2 + (64 * (16 * hf + tt) + (lane & 31)) * 8;
#pragma unroll
      for (int s = 0; s < 28; s++) {
        if (s + 2 < 28) RD_X(s + 2);
        if (hf == 0) {
          if (s == 2) {
            if (w == 0) poll_counters(p, 1 * 128 + g * 16, 16, 14u, lane);
            BAR();
          }
          if (s >= 2 && s < 9) xs[s - 2] = ld16s(rs, xso, WS_X1S + (16 + tt) * XB_SZ + (s - 2) * 8192);
          if (s >= 16 && s < 23) *(LAS v4u *)(S + LD_XB + XB_SZ + xlo + (s - 16) * 8192) = xs[s - 16];
          if (s == 14) csg_ = ld8(rs, cso);
          if (s == 10) warm = __builtin_amdgcn_raw_buffer_load_b32(rs, (tid < 448 ? tid : 447) * 128,
                                  WS_WDF + (2 * dtile + ((r / 7) >> 1)) * 112 * 1024 + ((r / 7) & 1) * 57344, SC1);
        } else {
          if (s == 12) {
            asm volatile("s_waitcnt vmcnt(12) ; PUBWAIT" ::: "memory");
            BAR();
            if (tid == 0) __hip_atomic_fetch_add((gu32 *)(p.ws + WS_CNT) + (256 + tt) * 64, 1u, RLX_AGENT);
          }
          wd[s] = ld16s(rs, lane16, wdo + ((s / 14) * 112 + s % 14) * 1024);
        }
        if (s == 20) { sg2[0] = ld8(rs, sto); sg2[1] = ld8(rs, sto + 256); }
        PIN();
        acc[0] = mfma32b(w1[s], fb0[s % 3], acc[0]);
        acc[1] = mfma32b(w1[s], fb1[s % 3], acc[1]);
        PIN();
        if (s == 3) STAMP(23 + 3 * hf);
        if (s == 27) STAMP(24 + 3 * hf);
      }
#undef RD_X
      if (hf == 0) {
        const u32 cs = finish_granule(rs, cso, tag, csg_);
        *(LAS u32 *)(S + LD_CSL + (w * 64 + lane) * 4) = cs;
        asm volatile("" ::: "memory");
      } else {
        if (w == 0) poll_counters(p, 256 + (t2 >> 1), 1, 14u, lane);
      }
      const u32 sv0 = finish_granule(rs, sto, tag, sg2[0]), sv1 = finish_granule(rs, sto + 256, tag, sg2[1]);
      const int T = 16 * hf + tt;
      STAMP(25 + 3 * hf);
#pragma unroll
      for (int j = 0; j < 2; j++) {
        const u32 sv = j ? sv1 : sv0;
        const float mu = bflo(sv), rsd = __builtin_amdgcn_rcpf(bfhi(sv));
        u32 hp[4];
#pragma unroll
        for (int gq = 0; gq < 2; gq++) {
          LAS unsigned char *cl = S + LD_CSL + w * 256 + (8 * gq + 4 * h) * 4;
          const f32x4 csg = *(LAS f32x4 *)(cl), csu = *(LAS f32x4 *)(cl + 64);
          const f32x4 bg = *(LAS f32x4 *)(cl + 128), bu = *(LAS f32x4 *)(cl + 192);
          float hv[4];
#pragma unroll
          for (int i = 0; i < 4; i++) {
            const float gg = rsd * (acc[j][4 * gq + i] - mu * csg[i]) + bg[i];
            const float uu = rsd * (acc[j][8 + 4 * gq + i] - mu * csu[i]) + bu[i];
            hv[i] = gg * uu;
          }
          hp[2 * gq] = pkbf(hv[0], hv[1]);
          hp[2 * gq + 1] = pkbf(hv[2], hv[3]);
        }
        st16s(rs, lane16, WS_HF + ((2 * T + j) * 112 + phi) * 1024, v4u{hp[0], hp[1], hp[2], hp[3]});
      }
      PIN();
      if (hf == 0) BAR();
      STAMP(10 + hf);
    }
  }

  asm volatile("" ::"v"(warm));
  BAR();
  v4u gbg[2];
#pragma unroll
  for (int hf = 0; hf < 2; hf++) {
    const int tau = 32 * hf + t2;
    if (hf == 1) {
      if (w == 0) poll_counters(p, 256 + 16 + (t2 >> 1), 1, 14u, lane);
      BAR();
    }
    STAMP(12 + 2 * hf);
    v4u hb[14];
#pragma unroll
    for (int k = 0; k < 14; k++) hb[k] = ld16s(rs, lane16, WS_HF + (tau * 112 + 14 * w + k) * 1024);
    const v4u res = ld16(rs, WS_X1T + ((tau >> 1) * D + 64 * dtile + ((tid & 255) >> 2)) * 128 + (tau & 1) * 64 + (tid & 3) * 16);
    if (hf == 0) {
      gbg[0] = ld16(rs, WS_MODG + (5 * D + 64 * dtile + 4 * (tid & 15)) * 8);
      gbg[1] = ld16(rs, WS_MODG + (5 * D + 64 * dtile + 4 * (tid & 15) + 2) * 8);
      asm volatile("s_waitcnt vmcnt(17) ; PUBWAIT" ::: "memory");
      BAR();
      if (tid == 0) __hip_atomic_fetch_add((gu32 *)(p.ws + WS_CNT) + (256 + 16 + tt) * 64, 1u, RLX_AGENT);
    }
    PIN();
    f32x16 acc[2];
#pragma unroll
    for (int j = 0; j < 2; j++)
#pragma unroll
      for (int i = 0; i < 16; i++) acc[j][i] = 0.f;
#pragma unroll
    for (int k = 0; k < 14; k++) {
      acc[0] = mfma32b(wd[k], hb[k], acc[0]);
      acc[1] = mfma32b(wd[14 + k], hb[k], acc[1]);
    }
    {
      const int col = lane & 31, h = lane >> 5;
      LAS unsigned char *rd = S + LD_RED + w * RED_W + col * RED_ROW + h * 16;
#pragma unroll
      for (int dl = 0; dl < 2; dl++)
#pragma unroll
        for (int gq = 0; gq < 4; gq++)
          *(LAS f32x4 *)(rd + dl * 128 + gq * 32) =
              f32x4{acc[dl][4 * gq], acc[dl][4 * gq + 1], acc[dl][4 * gq + 2], acc[dl][4 * gq + 3]};
      if (tid < 256) *(LAS v4u *)(S + LD_RES + (tid >> 2) * 64 + (tid & 3) * 16) = res;
    }
    BAR();
    {
      const int tok = tid >> 4, dq = tid & 15;
      f32x4 part[8];
#pragma unroll
      for (int ww = 0; ww < 8; ww++) part[ww] = *(LAS f32x4 *)(S + LD_RED + ww * RED_W + tok * RED_ROW + dq * 16);
      LAS u16 *rp = (LAS u16 *)(S + LD_RES) + (4 * dq) * 32 + tok;
      const u32 r0 = rp[0], r1 = rp[32], r2 = rp[64], r3 = rp[96];
      const f32x4 sum = ((part[0] + part[1]) + (part[2] + part[3])) + ((part[4] + part[5]) + (part[6] + part[7]));
      const float o0 = bf2f(r0) + asf(gbg[0].x) * sum[0];
      const float o1 = bf2f(r1) + asf(gbg[0].z) * sum[1];
      const float o2 = bf2f(r2) + asf(gbg[1].x) * sum[2];
      const float o3 = bf2f(r3) + asf(gbg[1].z) * sum[3];
      const int l = 32 * tau + tok;
      *(GAS v2u *)(p.out + l * D + 64 * dtile + 4 * dq) = v2u{pkbf(o0, o1), pkbf(o2, o3)};
    }
    STAMP(13 + 2 * hf);
    if (hf == 0) BAR();
  }
}

__device__ __forceinline__ void side_role(const Params &p, LAS unsigned char *S, const rsrc_t rs, const int tid,
                                          const int lane, const int w, const int b, const u32 epoch, v4u xr) {
  const int g = b & 7, sidx = 4 * g + (b >> 3) - 28;
  const int sc = sidx < 28 ? sidx : 27;
  const int lane16 = lane * 16;
  const u32 tag = __builtin_amdgcn_readfirstlane(epoch) + 1u;
  ln1_row(S, xr, w, lane);
  STAMP(1);
  BAR();
  publish_xt(S, rs, lane, w, tid, b, tag);
  STAMP(3);
  {
    const int dd = tid < D ? tid : 0;
    const int o1 = WS_WRDY + (tid < 224 ? tid : 0) * 128, o2 = WS_MODG + (4 * D + dd) * 8, o3 = WS_MODG + (3 * D + dd) * 8;
    const v2u g2 = ld8(rs, o2), g3 = ld8(rs, o3);
    const u32 vcb = finish_granule(rs, o2, tag, g2), vsb = finish_granule(rs, o3, tag, g3);
    if (tid < D) {
      ((LAS u16 *)(S + LD_CV))[tid] = (u16)(pkbf(1.0f + asf(vcb), 0.f) & 0xFFFFu);
      ((LAS u16 *)(S + LD_CV))[D + tid] = (u16)(vsb >> 16);
    }
#pragma unroll 1
    for (unsigned sp = 0; sp < SPIN_CAP; sp++) {
      const u32 wr = __builtin_amdgcn_raw_buffer_load_b32(rs, o1, 0, SC1);
      if (__all(wr == tag)) break;
      __builtin_amdgcn_s_sleep(2);
    }
    __builtin_amdgcn_fence(__ATOMIC_ACQUIRE, "wavefront");
  }
  BAR();
  STAMP(5);
  {
    const int ph = 4 * sc + (w >> 1), s0 = 14 * (w & 1);
    v4u wf[14];
#pragma unroll
    for (int k = 0; k < 14; k++) wf[k] = ld16s(rs, lane16, WS_W1F + (ph * 28 + s0 + k) * 1024);
    PIN();
    f32x16 acc;
#pragma unroll
    for (int i = 0; i < 16; i++) acc[i] = 0.f;
    LAS unsigned char *cvp = S + LD_CV + ((lane & 31) == 1 ? D * 2 : 0) + (lane >> 5) * 16;
#pragma unroll
    for (int k = 0; k < 14; k++) acc = mfma32b(wf[k], *(LAS v4u *)(cvp + (s0 + k) * 32), acc);
    LAS float *pr = (LAS float *)(S) + w * 64;
    if ((lane & 31) < 2) {
#pragma unroll
      for (int i = 0; i < 16; i++) pr[(lane & 31) * 32 + (i & 3) + 8 * (i >> 2) + 4 * (lane >> 5)] = acc[i];
    }
  }
  BAR();
  if (sidx < 28 && tid < 256) {
    const int pl = tid >> 6, e = tid & 63;
    const float v = ((LAS float *)S)[(2 * pl) * 64 + e] + ((LAS float *)S)[(2 * pl + 1) * 64 + e];
    st8(rs, WS_CSB + ((4 * sidx + pl) * 64 + e) * 8, v2u{__builtin_bit_cast(u32, v), tag});
  }
  STAMP(6);
#pragma unroll 1
  for (int hf = 0; hf < 2; hf++) {
    if (w == 0) poll_counters(p, hf * 128 + g * 16, 16, 14u, lane);
    BAR();
    const int tau = 32 * hf + sidx;
    const int tp = lane & 15, rgp = (lane >> 4) + 4 * w;
    u32 xv[14];
#pragma unroll
    for (int i = 0; i < 14; i++)
      xv[i] = __builtin_amdgcn_raw_buffer_load_b32(rs, rgp * 128 + tp * 4, WS_X1T + ((tau >> 1) * D + 32 * i) * 128 + (tau & 1) * 64, SC1);
    PIN();
    float s0 = 0.f, s1 = 0.f, q0 = 0.f, q1 = 0.f;
#pragma unroll
    for (int i = 0; i < 14; i++) {
      const float a = bflo(xv[i]), c = bfhi(xv[i]);
      s0 += a; s1 += c; q0 += a * a; q1 += c * c;
    }
    LAS f32x4 *pr = (LAS f32x4 *)(S + 4096);
    pr[rgp * 16 + tp] = f32x4{s0, s1, q0, q1};
    BAR();
    if (tid < 128) {
      const int tok = tid >> 2, part = tid & 3;
      float s = 0.f, q = 0.f;
#pragma unroll
      for (int i = 0; i < 8; i++) {
        const f32x4 v = pr[(part * 8 + i) * 16 + (tok >> 1)];
        s += (tok & 1) ? v[1] : v[0];
        q += (tok & 1) ? v[3] : v[2];
      }
      s += dpp0<0xB1, 0xF>(s); q += dpp0<0xB1, 0xF>(q);
      s += dpp0<0x4E, 0xF>(s); q += dpp0<0x4E, 0xF>(q);
      const float mean = s * (1.0f / D);
      const float var = rbf(fmaxf(q * (1.0f / D) - mean * mean, 0.f));
      const float sd = rbf(__builtin_amdgcn_sqrtf(rbf(var + 1e-5f)));
      if (part == 0) st8(rs, WS_STAT2 + (32 * tau + tok) * 8, v2u{pkbf(mean, sd), tag});
    }
    STAMP(8 + hf);
  }
}

__global__ void __launch_bounds__(NTHR, 2) k_main(Params p) {
  extern __shared__ __attribute__((aligned(16))) unsigned char smem[];
  LAS unsigned char *S = (LAS unsigned char *)smem;
  const int tid = threadIdx.x, lane = tid & 63, b = blockIdx.x;
  const int w = __builtin_amdgcn_readfirstlane(tid >> 6);
  const rsrc_t rs = __builtin_amdgcn_make_buffer_rsrc((void *)p.ws, 0, WS_END, 0x00020000);
  u32 pf;
  {
    unsigned long long pc;
    asm volatile("s_getpc_b64 %0" : "=s"(pc));
    pf = *(const GAS u32 *)((pc & ~127ull) + (unsigned)(tid < PF_LINES ? tid : 0) * 128u);
  }
  const v4u xr = *(const GAS v4u *)(p.x + (8 * b + w) * D + (lane < 56 ? lane : 55) * 8);
  const u32 epoch = ld8(rs, WS_EPOCH).x;
  u32 tw = __builtin_amdgcn_raw_buffer_load_b32(rs, (tid & 63) * (WS_DBG / 64 & ~127), 0, 0);
  tw ^= *(const GAS u32 *)(p.out + (tid & 7) * (L * D / 8));
  STAMP(0);
  if ((b >> 3) < 28) main_role(p, S, rs, tid, lane, w, b, epoch, xr);
  else side_role(p, S, rs, tid, lane, w, b, epoch, xr);
  asm volatile("" ::"v"(pf));
  asm volatile("" ::"v"(tw));
}

}

extern "C" void kernel_launch(void *const *d_in, const int *in_sizes, int n_in, void *d_out, int out_size, void *d_ws,
                              size_t ws_size, hipStream_t stream) {
  using namespace mk;
  Params p;
  p.x = (const u16 *)d_in[0];
  p.Are = (const u16 *)d_in[1]; p.Aim = (const u16 *)d_in[2];
  p.Bre = (const u16 *)d_in[3]; p.Bim = (const u16 *)d_in[4];
  p.Cre = (const u16 *)d_in[5]; p.Cim = (const u16 *)d_in[6];
  p.logdt = (const u16 *)d_in[7]; p.Dsk = (const u16 *)d_in[8];
  p.wg = (const u16 *)d_in[10]; p.wu = (const u16 *)d_in[11]; p.wd = (const u16 *)d_in[12];
  p.cvec = (const u16 *)d_in[13]; p.wmod = (const u16 *)d_in[14];
  p.out = (u16 *)d_out;
  p.ws = (unsigned char *)d_ws;
  (void)hipFuncSetAttribute((const void *)k_main, hipFuncAttributeMaxDynamicSharedMemorySize, LDS_BYTES);
  k_main<<<NWG, NTHR, LDS_BYTES, stream>>>(p);
}
```

```cpp
#include <hip/hip_runtime.h>
#include <stdint.h>

namespace mk {

typedef unsigned short u16;
typedef unsigned u32;
typedef u32 v4u __attribute__((ext_vector_type(4)));
typedef u32 v2u __attribute__((ext_vector_type(2)));
typedef float f2 __attribute__((ext_vector_type(2)));
typedef float f32x4 __attribute__((ext_vector_type(4)));
typedef float f32x16 __attribute__((ext_vector_type(16)));
typedef _Float16 h2 __attribute__((ext_vector_type(2)));
typedef _Float16 h8 __attribute__((ext_vector_type(8)));
typedef __bf16 b2 __attribute__((ext_vector_type(2)));
typedef __bf16 b8 __attribute__((ext_vector_type(8)));
typedef short s4 __attribute__((ext_vector_type(4)));
#define LAS __attribute__((address_space(3)))
#define GAS __attribute__((address_space(1)))
typedef GAS u32 gu32;
#define RLX_AGENT __ATOMIC_RELAXED, __HIP_MEMORY_SCOPE_AGENT

constexpr int L = 2048, D = 448, NS = 256, F = 1792;
constexpr int NWG = 256, NTHR = 512;
constexpr int SC1 = 16;
constexpr int PF_LINES = 212;

constexpr int WS_EPOCH = 0;
constexpr int WS_CNT = 4096;
constexpr int NCNT = 288;
constexpr int WS_STAT1 = WS_CNT + NCNT * 256;
constexpr int WS_MODG = WS_STAT1 + 2048 * 8;
constexpr int WS_CSB = WS_MODG + 2688 * 8;
constexpr int WS_STAT2 = WS_CSB + 112 * 64 * 8;
constexpr int WS_WRDY = WS_STAT2 + 2048 * 8;
constexpr int WS_XG = WS_WRDY + 256 * 128;
constexpr int WS_W1F = WS_XG + 128 * 224 * 128;
constexpr int WS_WDF = WS_W1F + 112 * 28 * 1024;
constexpr int WS_X1T = WS_WDF + 14 * 112 * 1024;
constexpr int WS_X1S = WS_X1T + 32 * 448 * 128;
constexpr int WS_HF = WS_X1S + 32 * 448 * 128;
constexpr int WS_DBG = WS_HF + 64 * 112 * 1024;
constexpr int WS_END = WS_DBG + (8 << 20);
static_assert(WS_STAT1 % 128 == 0 && WS_MODG % 128 == 0 && WS_CSB % 128 == 0 && WS_STAT2 % 128 == 0 && WS_XG % 128 == 0 && WS_W1F % 128 == 0, "align");

constexpr int PQ_ROW = 1056, PQ_CH = 57 * PQ_ROW;
constexpr int LD_PQ = 0;
constexpr int LD_KC = 0;
constexpr int KC_CP = 4160, KC_CH = 4 * KC_CP;
constexpr int LD_EX = 33280;
constexpr int LD_XB = 0;
constexpr int XB_SZ = 448 * 128;
constexpr int LD_RED = 0;
constexpr int RED_ROW = 272, RED_W = 32 * RED_ROW;
constexpr int LD_RES = 8 * RED_W;
constexpr int LD_KD = 120384;
constexpr int KD_CH = 784 * 2;
constexpr int LD_U = LD_KD + 2 * KD_CH;
constexpr int U_CH = 2304 * 2;
constexpr int LD_XS = LD_U + 2 * U_CH;
constexpr int LD_XROW = 120384;
constexpr int LD_WSTG = LD_XROW + 16128;
constexpr int WSTG_ROW = 464, WSTG_M = 16 * WSTG_ROW;
constexpr int LD_SMALL = LD_WSTG + 3 * WSTG_M;
constexpr int LD_STAT = LD_SMALL;
constexpr int LD_WMAX = LD_SMALL + 64;
constexpr int LD_CSL = LD_SMALL + 96;
constexpr int LD_STAMP = LD_CSL + 2048;
constexpr int LD_CV = LD_STAMP + 128;
constexpr int LD_END = LD_CV + 2 * 448 * 2;
constexpr int LDS_BYTES = 160 * 1024;
static_assert(LD_XS + 8192 <= LD_SMALL && LD_END <= LDS_BYTES && LD_CSL % 16 == 0 && LD_CV % 16 == 0, "lds");
static_assert(LD_RES + 4096 <= LD_KD, "lds red");

struct Params {
  const u16 *x, *Are, *Aim, *Bre, *Bim, *Cre, *Cim, *logdt, *Dsk, *wg, *wu, *wd, *cvec, *wmod;
  u16 *out;
  unsigned char *ws;
};

struct KTab { u16 v[256][16]; };
constexpr int bf16_round_int(int j) {
  if (j < 256) return j;
  int sh = 0; int t = j; while (t >= 256) { t >>= 1; sh++; }
  int q = j >> sh, rem = j & ((1 << sh) - 1), half = 1 << (sh - 1);
  if (rem > half || (rem == half && (q & 1))) q++;
  return q << sh;
}
constexpr int kd_index(int j) {
  if (j < 0) return 768;
  int l = bf16_round_int(j);
  if (l >= 2048) return 40 * 16;
  int k = l < 256 ? 0 : (l < 512 ? 1 : (l < 1024 ? 2 : 3));
  int base = k ? (128 << k) : 0;
  int rem = l - base;
  int a = rem >> 7, b = (rem >> 3) & 15, c = (rem & 7) >> k;
  int row = (k ? 8 + 8 * k : 0) + a * (8 >> k) + c;
  return row * 16 + b;
}
constexpr KTab make_ktab() {
  KTab t{};
  for (int i = 0; i < 256; i++)
    for (int j = 0; j < 16; j++) t.v[i][j] = (u16)(j < 11 ? kd_index(8 * i - 3 + j) : 768);
  return t;
}
__device__ const KTab g_ktab = make_ktab();

__device__ __forceinline__ float asf(u32 v) { return __builtin_bit_cast(float, v); }
__device__ __forceinline__ h2 ash2(u32 v) { return __builtin_bit_cast(h2, v); }
__device__ __forceinline__ float bf2f(u32 v) { return __builtin_bit_cast(float, v << 16); }
__device__ __forceinline__ float bflo(u32 v) { return __builtin_bit_cast(float, v << 16); }
__device__ __forceinline__ float bfhi(u32 v) { return __builtin_bit_cast(float, v & 0xFFFF0000u); }
__device__ __forceinline__ u32 pkbf(float a, float b) {
  return __builtin_bit_cast(u32, __builtin_convertvector(f2{a, b}, b2));
}
__device__ __forceinline__ u32 pkh(float a, float b) {
  return __builtin_bit_cast(u32, __builtin_convertvector(f2{a, b}, h2));
}
__device__ __forceinline__ float rbf(float a) { return bflo(pkbf(a, a)); }
__device__ __forceinline__ f2 cmul(f2 a, f2 b) {
  f2 t = f2{a.x, a.x} * b;
  return __builtin_elementwise_fma(f2{a.y, a.y}, f2{-b.y, b.x}, t);
}
template <int CTRL, int RM> __device__ __forceinline__ float dpp0(float v) {
  return __builtin_bit_cast(float, __builtin_amdgcn_update_dpp(0, __builtin_bit_cast(int, v), CTRL, RM, 0xF, true));
}
__device__ __forceinline__ float wave_sum(float v) {
  v += dpp0<0x111, 0xF>(v);
  v += dpp0<0x112, 0xF>(v);
  v += dpp0<0x114, 0xF>(v);
  v += dpp0<0x118, 0xF>(v);
  v += dpp0<0x142, 0xA>(v);
  v += dpp0<0x143, 0xC>(v);
  return __builtin_bit_cast(float, __builtin_amdgcn_readlane(__builtin_bit_cast(int, v), 63));
}
__device__ __forceinline__ float wave_max_nonneg(float v) {
  v = fmaxf(v, dpp0<0x111, 0xF>(v));
  v = fmaxf(v, dpp0<0x112, 0xF>(v));
  v = fmaxf(v, dpp0<0x114, 0xF>(v));
  v = fmaxf(v, dpp0<0x118, 0xF>(v));
  v = fmaxf(v, dpp0<0x142, 0xA>(v));
  v = fmaxf(v, dpp0<0x143, 0xC>(v));
  return __builtin_bit_cast(float, __builtin_amdgcn_readlane(__builtin_bit_cast(int, v), 63));
}
__device__ __forceinline__ v2u trread(LAS unsigned char *p) {
  return __builtin_bit_cast(v2u, __builtin_amdgcn_ds_read_tr16_b64_v4i16((LAS s4 *)p));
}
__device__ __forceinline__ v4u tr8(LAS unsigned char *p0, LAS unsigned char *p1) {
  v2u a = trread(p0), b = trread(p1);
  return v4u{a.x, a.y, b.x, b.y};
}
__device__ __forceinline__ f32x4 mfma16h(v4u a, v4u b, f32x4 c) {
  return __builtin_amdgcn_mfma_f32_16x16x32_f16(__builtin_bit_cast(h8, a), __builtin_bit_cast(h8, b), c, 0, 0, 0);
}
__device__ __forceinline__ f32x16 mfma32b(v4u a, v4u b, f32x16 c) {
  return __builtin_amdgcn_mfma_f32_32x32x16_bf16(__builtin_bit_cast(b8, a), __builtin_bit_cast(b8, b), c, 0, 0, 0);
}
#define BAR() asm volatile("s_waitcnt lgkmcnt(0)\n\ts_barrier" ::: "memory")
#define DRAIN() asm volatile("s_waitcnt vmcnt(0)" ::: "memory")
#define PIN() do { asm volatile("" ::: "memory"); __builtin_amdgcn_sched_barrier(0); } while (0)
typedef __amdgpu_buffer_rsrc_t rsrc_t;
__device__ __forceinline__ v4u ld16(rsrc_t rs, int off) { return __builtin_amdgcn_raw_buffer_load_b128(rs, off, 0, SC1); }
__device__ __forceinline__ v4u ld16s(rsrc_t rs, int voff, int soff) { return __builtin_amdgcn_raw_buffer_load_b128(rs, voff, soff, SC1); }
__device__ __forceinline__ void st16s(rsrc_t rs, int voff, int soff, v4u v) { __builtin_amdgcn_raw_buffer_store_b128(v, rs, voff + soff, 0, SC1); }
__device__ __forceinline__ v2u ld8(rsrc_t rs, int off) { return __builtin_amdgcn_raw_buffer_load_b64(rs, off, 0, SC1); }
__device__ __forceinline__ void st16(rsrc_t rs, int off, v4u v) { __builtin_amdgcn_raw_buffer_store_b128(v, rs, off, 0, SC1); }
__device__ __forceinline__ void st8(rsrc_t rs, int off, v2u v) { __builtin_amdgcn_raw_buffer_store_b64(v, rs, off, 0, SC1); }
__device__ __forceinline__ void st4(rsrc_t rs, int off, u32 v) { __builtin_amdgcn_raw_buffer_store_b32(v, rs, off, 0, SC1); }

constexpr unsigned SPIN_CAP = 1u << 14;

#define STAMP(i)

__device__ __forceinline__ void poll_counters(const Params &p, int idx0, int ns, u32 want, int lane) {
  gu32 *cw = (gu32 *)(p.ws + WS_CNT) + (idx0 + (lane < ns ? lane : 0)) * 64;
#pragma unroll 1
  for (unsigned sp = 0; sp < SPIN_CAP; sp++) {
    const u32 v = __hip_atomic_load(cw, RLX_AGENT);
    if (__all(v >= want)) break;
    __builtin_amdgcn_s_sleep(1);
  }
  __builtin_amdgcn_fence(__ATOMIC_ACQUIRE, "wavefront");
}
__device__ __forceinline__ u32 finish_granule(rsrc_t rs, int off, u32 tag, v2u g) {
#pragma unroll 1
  for (unsigned sp = 0; sp < SPIN_CAP; sp++) {
    if (__all(g.y == tag)) break;
    __builtin_amdgcn_s_sleep(1);
    g = ld8(rs, off);
  }
  __builtin_amdgcn_fence(__ATOMIC_ACQUIRE, "wavefront");
  return g.x;
}

template <int ROW> __device__ __forceinline__ void pqw4(u32 m0v, f2 a, f2 b, f2 c, f2 d) {
  const u32 h0 = pkh(a.x, a.y), h1 = pkh(b.x, b.y), h2_ = pkh(c.x, c.y), h3 = pkh(d.x, d.y);
  asm volatile("s_mov_b32 m0, %4\n\ts_nop 0\n\tds_write_addtid_b32 %0 offset:%c5\n\tds_write_addtid_b32 %1 offset:%c6\n\t"
               "ds_write_addtid_b32 %2 offset:%c7\n\tds_write_addtid_b32 %3 offset:%c8"
               ::"v"(h0), "v"(h1), "v"(h2_), "v"(h3), "s"(m0v), "i"(ROW * PQ_ROW), "i"((ROW + 1) * PQ_ROW),
               "i"((ROW + 2) * PQ_ROW), "i"((ROW + 3) * PQ_ROW) : "memory");
}
template <int ROW> __device__ __forceinline__ void pqw1(u32 m0v, f2 a) {
  const u32 h0 = pkh(a.x, a.y);
  asm volatile("s_mov_b32 m0, %1\n\ts_nop 0\n\tds_write_addtid_b32 %0 offset:%c2" ::"v"(h0), "s"(m0v), "i"(ROW * PQ_ROW) : "memory");
}
__device__ __forceinline__ f2 zpow(float al, float rv) {
  const float e = __builtin_amdgcn_exp2f(al);
  const float r = __builtin_amdgcn_fractf(rv);
  return f2{e * __builtin_amdgcn_cosf(r), e * __builtin_amdgcn_sinf(r)};
}
__device__ __forceinline__ float pow2b(int e) { return asf((u32)e << 23); }

__device__ __forceinline__ void ln1_row(LAS unsigned char *S, v4u xr, int row, int lane) {
  float xv[8] = {bflo(xr.x), bfhi(xr.x), bflo(xr.y), bfhi(xr.y), bflo(xr.z), bfhi(xr.z), bflo(xr.w), bfhi(xr.w)};
  float s = ((xv[0] + xv[1]) + (xv[2] + xv[3])) + ((xv[4] + xv[5]) + (xv[6] + xv[7]));
  s = lane < 56 ? s : 0.f;
  const float mean = wave_sum(s) * (1.0f / D);
  float q = 0.f;
#pragma unroll
  for (int i = 0; i < 8; i++) { const float e = xv[i] - mean; q += e * e; }
  q = lane < 56 ? q : 0.f;
  const float var = rbf(wave_sum(q) * (1.0f / D));
  const float sd = rbf(__builtin_amdgcn_sqrtf(rbf(var + 1e-5f)));
  if (lane == 0) *(LAS u32 *)(S + LD_STAT + row * 4) = pkbf(mean, sd);
  if (lane < 56) *(LAS v4u *)(S + LD_XROW + row * 960 + lane * 16) = xr;
}
__device__ __forceinline__ void publish_xt(LAS unsigned char *S, const rsrc_t rs, int lane, int w, int tau, u32 tag) {
  const int G = lane >> 4, e = (lane >> 2) & 3, pp = lane & 3;
  LAS unsigned char *ax = S + LD_XROW + (4 * G + e) * 960 + 4 * pp * 2;
  const int go = WS_XG + tau * (224 * 128) + (lane & 15) * 64 + G * 16;
  v2u v[4];
#pragma unroll
  for (int i = 0; i < 4; i++) v[i] = trread(ax + (w + 8 * (i < 3 || w < 4 ? i : 2)) * 32);
#pragma unroll
  for (int i = 0; i < 4; i++)
    if (i < 3 || w < 4) st16(rs, go + (w + 8 * i) * 1024, v4u{v[i].x, tag, v[i].y, tag});
  if (w == 7 && lane < 16) st8(rs, WS_STAT1 + (16 * tau + lane) * 8, v2u{*(LAS u32 *)(S + LD_STAT + lane * 4), tag});
}

__device__ __forceinline__ void main_role(const Params &p, LAS unsigned char *S, const rsrc_t rs, const int tid,
                                          const int lane, const int w, const int b, const u32 epoch) {
  const int g = b & 7, r = b >> 3, m = 28 * g + r;
  const int cch = w >> 2, t8 = tid & 255, dch = 2 * m + cch;
  const int lane16 = lane * 16;
  const bool xprod = (r & 1) == 0;
  const int tau = 16 * g + (r >> 1), mi = 14 * g + (r >> 1);
  const int tm = tid < 504 ? tid : 503, mrow = tm / 3, mpc = tm % 3;
  v4u rl[3];
  u32 cv[3];
#pragma unroll
  for (int i = 0; i < 3; i++) {
    const int k = mrow + 168 * i < D ? mrow + 168 * i : D - 1;
    const u16 *ax = p.x + (16 * tau + 2 * w + (i ? 1 : 0)) * D + (lane < 56 ? lane : 55) * 8;
    const u16 *aw = p.wmod + k * (6 * D) + 24 * mi + 8 * mpc;
    rl[i] = *(const GAS v4u *)(xprod ? ax : aw);
    cv[i] = p.cvec[k];
  }
  const int po = dch * NS + t8;
  const u32 pAre = p.Are[po], pAim = p.Aim[po], pBre = p.Bre[po], pBim = p.Bim[po], pCre = p.Cre[po], pCim = p.Cim[po];
  const u32 pLdt = p.logdt[dch], pDsk = p.Dsk[dch];
  PIN();
  const int tc = tid < 448 ? tid : 447, srow = tc / 28, scol = tc % 28;
  const v4u ws0 = *(const GAS v4u *)(p.wg + (16 * (m >> 3) + srow) * F + 224 * (m & 7) + 8 * scol);
  const v4u ws1 = *(const GAS v4u *)(p.wu + (16 * (m >> 3) + srow) * F + 224 * (m & 7) + 8 * scol);
  const v4u ws2 = *(const GAS v4u *)(p.wd + (16 * (m >> 1) + srow) * D + 224 * (m & 1) + 8 * scol);
  const v4u kt0 = *(const GAS v4u *)(&g_ktab.v[t8][0]);
  const v4u kt1 = *(const GAS v4u *)(&g_ktab.v[t8][8]);
  PIN();
  asm volatile("" : "+v"(rl[0]), "+v"(rl[1]), "+v"(rl[2]));
  const u32 tag = __builtin_amdgcn_readfirstlane(epoch) + 1u;

  if (xprod) {
    ln1_row(S, rl[0], 2 * w, lane);
    ln1_row(S, rl[1], 2 * w + 1, lane);
  } else {
    float a[8] = {0.f, 0.f, 0.f, 0.f, 0.f, 0.f, 0.f, 0.f};
#pragma unroll
    for (int i = 0; i < 3; i++) {
      const float cx = bf2f(cv[i]);
      float sl = rbf(cx * __builtin_amdgcn_rcpf(1.0f + __expf(-cx)));
      sl = mrow + 168 * i < D ? sl : 0.f;
      a[0] += sl * bflo(rl[i].x); a[1] += sl * bfhi(rl[i].x); a[2] += sl * bflo(rl[i].y); a[3] += sl * bfhi(rl[i].y);
      a[4] += sl * bflo(rl[i].z); a[5] += sl * bfhi(rl[i].z); a[6] += sl * bflo(rl[i].w); a[7] += sl * bfhi(rl[i].w);
    }
    if (tid < 504) {
      LAS f32x4 *mp = (LAS f32x4 *)(S + LD_XROW + mrow * 96 + mpc * 32);
      mp[0] = f32x4{a[0], a[1], a[2], a[3]}; mp[1] = f32x4{a[4], a[5], a[6], a[7]};
    }
  }
  STAMP(1);
  BAR();
  if (xprod) {
    publish_xt(S, rs, lane, w, tau, tag);
  } else if (w < 6) {
    const int col = tid >> 4, part = tid & 15;
    float sm = 0.f;
#pragma unroll
    for (int i = 0; i < 11; i++) {
      const int rr = part + 16 * i;
      const float v = ((LAS float *)(S + LD_XROW))[(rr < 168 ? rr : 0) * 24 + col];
      sm += rr < 168 ? v : 0.f;
    }
    sm += dpp0<0x111, 0xF>(sm); sm += dpp0<0x112, 0xF>(sm); sm += dpp0<0x114, 0xF>(sm); sm += dpp0<0x118, 0xF>(sm);
    if (part == 15) st8(rs, WS_MODG + (24 * mi + col) * 8, v2u{__builtin_bit_cast(u32, rbf(sm)), tag});
  }
  PIN();
  STAMP(2);

  const float dt = rbf(__expf(bf2f(pLdt)));
  const float al = dt * bf2f(pAre) * 1.44269504088896f;
  const float rv = dt * bf2f(pAim) * 0.159154943091895f;
  const f2 Wc = cmul(f2{bf2f(pCre), bf2f(pCim)}, f2{dt * bf2f(pBre), dt * bf2f(pBim)});
  {
    const float mx = wave_max_nonneg(fmaxf(fabsf(Wc.x), fabsf(Wc.y)));
    if (lane == 0) *(LAS float *)(S + LD_WMAX + w * 4) = mx;
  }
  const u32 m0v = (u32)(LD_PQ + (w >> 2) * PQ_CH + (w & 3) * 256);
  const f2 p1 = zpow(al, rv), z8 = zpow(al * 8.f, rv * 8.f), Z1 = zpow(al * 128.f, rv * 128.f);
  const f2 p2 = cmul(p1, p1), p3 = cmul(p2, p1), p4 = cmul(p2, p2), p5 = cmul(p4, p1), p6 = cmul(p4, p2), p7 = cmul(p4, p3);
  pqw4<0>(m0v, f2{1.f, 0.f}, p1, p2, p3);
  pqw4<4>(m0v, p4, p5, p6, p7);
  pqw4<8>(m0v, Z1, cmul(Z1, p1), cmul(Z1, p2), cmul(Z1, p3));
  pqw4<12>(m0v, cmul(Z1, p4), cmul(Z1, p5), cmul(Z1, p6), cmul(Z1, p7));
  if (tid < 448) {
    *(LAS v4u *)(S + LD_WSTG + 0 * WSTG_M + srow * WSTG_ROW + scol * 16) = ws0;
    *(LAS v4u *)(S + LD_WSTG + 1 * WSTG_M + srow * WSTG_ROW + scol * 16) = ws1;
    *(LAS v4u *)(S + LD_WSTG + 2 * WSTG_M + srow * WSTG_ROW + scol * 16) = ws2;
  }
  BAR();
  {
    const int G = lane >> 4, e = (lane >> 2) & 3, pp = lane & 3, h = G >> 1;
    const int w7 = w < 7 ? w : 6, j1 = w + 8 < 14 ? w + 8 : 13;
    LAS unsigned char *a0 = S + LD_WSTG + (G & 1) * WSTG_M + (8 * h + e) * WSTG_ROW + (16 * w + 4 * pp) * 2;
    LAS unsigned char *a1 = S + LD_WSTG + (G & 1) * WSTG_M + (8 * h + e) * WSTG_ROW + (16 * j1 + 4 * pp) * 2;
    LAS unsigned char *a2 = S + LD_WSTG + 2 * WSTG_M + (4 * h + e) * WSTG_ROW + (32 * w7 + 16 * (G & 1) + 4 * pp) * 2;
    const v4u v0 = tr8(a0, a0 + 4 * WSTG_ROW), v1 = tr8(a1, a1 + 4 * WSTG_ROW), v2 = tr8(a2, a2 + 8 * WSTG_ROW);
    st16s(rs, lane16, WS_W1F + ((14 * (m & 7) + w) * 28 + (m >> 3)) * 1024, v0);
    if (w < 6) st16s(rs, lane16, WS_W1F + ((14 * (m & 7) + w + 8) * 28 + (m >> 3)) * 1024, v1);
    if (w < 7) st16s(rs, lane16, WS_WDF + ((7 * (m & 1) + w) * 112 + (m >> 1)) * 1024, v2);
    if (w == 7 && lane < 2 && m + 224 * lane < NCNT) st4(rs, WS_CNT + (m + 224 * lane) * 256, 0u);
  }
  PIN();
  STAMP(3);
  int ebW;
  {
    const f32x4 mx4 = *(LAS f32x4 *)(S + LD_WMAX + (w & 4) * 4);
    const float mx = fmaxf(fmaxf(mx4[0], mx4[1]), fmaxf(mx4[2], mx4[3]));
    ebW = (int)((__builtin_bit_cast(u32, mx) >> 23) & 0xFF);
    ebW = ebW < 14 ? 14 : (ebW > 253 ? 253 : ebW);
  }
  const float sQ = pow2b(267 - ebW);
  {
    const f2 Z2 = cmul(Z1, Z1), Z3 = cmul(Z2, Z1), Z4 = cmul(Z2, Z2);
    pqw4<16>(m0v, Z2, cmul(Z2, p2), cmul(Z2, p4), cmul(Z2, p6));
    pqw4<20>(m0v, Z3, cmul(Z3, p2), cmul(Z3, p4), cmul(Z3, p6));
    const f2 Z5 = cmul(Z4, Z1), Z6 = cmul(Z4, Z2), Z7 = cmul(Z4, Z3), Z8 = cmul(Z4, Z4);
    pqw4<24>(m0v, Z4, cmul(Z4, p4), Z5, cmul(Z5, p4));
    pqw4<28>(m0v, Z6, cmul(Z6, p4), Z7, cmul(Z7, p4));
    pqw4<32>(m0v, Z8, cmul(Z8, Z1), cmul(Z8, Z2), cmul(Z8, Z3));
    pqw4<36>(m0v, cmul(Z8, Z4), cmul(Z8, Z5), cmul(Z8, Z6), cmul(Z8, Z7));
    pqw1<40>(m0v, cmul(Z8, Z8));
    const f2 z16 = cmul(z8, z8), z24 = cmul(z16, z8), z32 = cmul(z16, z16);
    f2 q0 = Wc * sQ, q1 = cmul(q0, z8), q2 = cmul(q0, z16), q3 = cmul(q0, z24);
#define NEGI(q) f2{(q).x, -(q).y}
    pqw4<41>(m0v, NEGI(q0), NEGI(q1), NEGI(q2), NEGI(q3));
    q0 = cmul(q0, z32); q1 = cmul(q1, z32); q2 = cmul(q2, z32); q3 = cmul(q3, z32);
    pqw4<45>(m0v, NEGI(q0), NEGI(q1), NEGI(q2), NEGI(q3));
    q0 = cmul(q0, z32); q1 = cmul(q1, z32); q2 = cmul(q2, z32); q3 = cmul(q3, z32);
    pqw4<49>(m0v, NEGI(q0), NEGI(q1), NEGI(q2), NEGI(q3));
    q0 = cmul(q0, z32); q1 = cmul(q1, z32); q2 = cmul(q2, z32); q3 = cmul(q3, z32);
    pqw4<53>(m0v, NEGI(q0), NEGI(q1), NEGI(q2), NEGI(q3));
#undef NEGI
  }
  v4u xg[2], sg[4], mg[4];
  const int hch = (tid >> 2) & 1, l4 = 16 * (tid >> 3) + 4 * (tid & 3);
  const int xo = WS_XG + ((tid >> 3) * 224 + m) * 128 + (tid & 7) * 16, so = WS_STAT1 + l4 * 8, mo = WS_MODG + 2 * m * 8;
#define LD_HANDOFF_A() do { \
    xg[0] = ld16(rs, xo); xg[1] = ld16(rs, xo + 64 * 224 * 128); \
    sg[0] = ld16(rs, so); sg[1] = ld16(rs, so + 16); sg[2] = ld16(rs, so + 8192); sg[3] = ld16(rs, so + 8192 + 16); \
    mg[0] = ld16(rs, mo + 0 * D * 8); mg[1] = ld16(rs, mo + 1 * D * 8); \
    mg[2] = ld16(rs, mo + 2 * D * 8); mg[3] = ld16(rs, mo + 4 * D * 8); } while (0)
  STAMP(20);
  LD_HANDOFF_A();
  PIN();
  STAMP(4);
  asm volatile("s_waitcnt vmcnt(10) ; PUBWAIT" ::: "memory");
  STAMP(21);
  BAR();
  if (tid == 0) st4(rs, WS_WRDY + m * 128, tag);
  STAMP(29);
  {
    const int tile = w & 3;
    if (tile < 3) {
      LAS unsigned char *pa = S + LD_PQ + cch * PQ_CH + (16 * tile + (lane & 15)) * PQ_ROW + (lane >> 4) * 16;
      LAS unsigned char *pb = S + LD_PQ + cch * PQ_CH + (41 + (lane & 15)) * PQ_ROW + (lane >> 4) * 16;
      f32x4 acc0 = {0.f, 0.f, 0.f, 0.f}, acc1 = acc0;
      v4u fa[16], fb[16];
#pragma unroll
      for (int ks = 0; ks < 16; ks++) { fa[ks] = *(LAS v4u *)(pa + ks * 64); fb[ks] = *(LAS v4u *)(pb + ks * 64); }
      PIN();
#pragma unroll
      for (int ks = 0; ks < 16; ks += 2) { acc0 = mfma16h(fa[ks], fb[ks], acc0); acc1 = mfma16h(fa[ks + 1], fb[ks + 1], acc1); }
      acc0 += acc1;
      LAS u16 *kd = (LAS u16 *)(S + LD_KD + cch * KD_CH) + (16 * tile + 4 * (lane >> 4)) * 16 + (lane & 15);
#pragma unroll
      for (int i = 0; i < 4; i++) kd[i * 16] = (u16)(pkh(acc0[i] * (1.0f / 256.0f), 0.f) & 0xFFFFu);
    } else if (lane < 16) {
      ((LAS u16 *)(S + LD_KD + cch * KD_CH))[768 + lane] = 0;
    }
  }
#pragma unroll 1
  for (unsigned sp = 0; sp < SPIN_CAP; sp++) {
    bool ok = xg[0].y == tag && xg[0].w == tag && xg[1].y == tag && xg[1].w == tag;
#pragma unroll
    for (int i = 0; i < 4; i++) ok = ok && sg[i].y == tag && sg[i].w == tag && mg[i].y == tag && mg[i].w == tag;
    if (__all(ok)) break;
    __builtin_amdgcn_s_sleep(1);
    LD_HANDOFF_A();
  }
#undef LD_HANDOFF_A
  __builtin_amdgcn_fence(__ATOMIC_ACQUIRE, "wavefront");
  STAMP(5);
  BAR();

  v4u w1[28];
  const int fc = 7 * (g & 1) + r % 7, tt = 4 * (g >> 1) + r / 7;
  const int phi = 8 * fc + w;
  const int w1o = WS_W1F + phi * 28 * 1024;
#define LDW1(s) do { w1[s] = ld16s(rs, lane16, w1o + (s) * 1024); PIN(); } while (0)

  {
    LAS u16 *kd = (LAS u16 *)(S + LD_KD + cch * KD_CH);
    u32 v[11];
    const u32 ix[6] = {kt0.x, kt0.y, kt0.z, kt0.w, kt1.x, kt1.y};
#pragma unroll
    for (int i = 0; i < 11; i++) v[i] = kd[(i & 1) ? (ix[i >> 1] >> 16) : (ix[i >> 1] & 0xFFFFu)];
    LAS unsigned char *kc = S + LD_KC + cch * KC_CH + (16 + 8 * t8) * 2;
#pragma unroll
    for (int e = 0; e < 4; e++) {
      v4u o;
      o.x = v[3 - e] | (v[4 - e] << 16);
      o.y = v[5 - e] | (v[6 - e] << 16);
      o.z = v[7 - e] | (v[8 - e] << 16);
      o.w = v[9 - e] | (v[10 - e] << 16);
      *(LAS v4u *)(kc + e * KC_CP) = o;
    }
    if (t8 < 8) *(LAS v4u *)(S + LD_KC + cch * KC_CH + (t8 >> 1) * KC_CP + (t8 & 1) * 16) = v4u{0, 0, 0, 0};
  }
  float A1, A2, c2;
  {
    int eb[2];
    float c1v[2], sav[2];
#pragma unroll
    for (int c = 0; c < 2; c++) {
      sav[c] = asf(c ? mg[0].z : mg[0].x);
      c1v[c] = rbf(1.0f + asf(c ? mg[1].z : mg[1].x));
      const int e0 = (int)((__builtin_bit_cast(u32, 21.5f * fabsf(c1v[c]) + fabsf(sav[c])) >> 23) & 0xFF);
      eb[c] = e0 < 14 ? 14 : (e0 > 253 ? 253 : e0);
    }
    const float sH = pow2b(267 - (hch ? eb[1] : eb[0]));
    const float c1s = (hch ? c1v[1] : c1v[0]) * sH, sas = (hch ? sav[1] : sav[0]) * sH;
#pragma unroll
    for (int i = 0; i < 2; i++) {
      const u32 x01 = xg[i].x, x23 = xg[i].z;
      const u32 s0 = sg[2 * i].x, s1 = sg[2 * i].z, s2 = sg[2 * i + 1].x, s3 = sg[2 * i + 1].z;
      const float h0 = ((bflo(x01) - bflo(s0)) * __builtin_amdgcn_rcpf(bfhi(s0))) * c1s + sas;
      const float h1 = ((bfhi(x01) - bflo(s1)) * __builtin_amdgcn_rcpf(bfhi(s1))) * c1s + sas;
      const float h2_ = ((bflo(x23) - bflo(s2)) * __builtin_amdgcn_rcpf(bfhi(s2))) * c1s + sas;
      const float h3 = ((bfhi(x23) - bflo(s3)) * __builtin_amdgcn_rcpf(bfhi(s3))) * c1s + sas;
      const int l0 = l4 + 1024 * i;
      *(LAS v2u *)(S + LD_U + hch * U_CH + (256 + l0) * 2) = v2u{pkh(h0, h1), pkh(h2_, h3)};
      *(LAS v2u *)(S + LD_XS + hch * 4096 + l0 * 2) = v2u{x01, x23};
    }
    if (t8 < 32) *(LAS v4u *)(S + LD_U + cch * U_CH + 16 * t8) = v4u{0, 0, 0, 0};
    const float ga = asf(cch ? mg[2].z : mg[2].x);
    const float iH = pow2b((cch ? eb[1] : eb[0]) - 13);
    A1 = ga * iH * pow2b(ebW - 5);
    A2 = ga * bf2f(pDsk) * iH;
    c2 = rbf(1.0f + asf(cch ? mg[3].z : mg[3].x));
  }
  const int fo = WS_WRDY + t8 * 128;
  u32 wr = __builtin_amdgcn_raw_buffer_load_b32(rs, fo, 0, SC1);
  STAMP(6);
  BAR();

  {
    const int c = cch, pq = w & 3;
    const int a15 = lane & 15, q = lane >> 4, e = (lane >> 2) & 3, pp = lane & 3;
    LAS unsigned char *ka = S + LD_KC + c * KC_CH + e * KC_CP + (16 + 16 * (q >> 1) - 8 * (q & 1) + 4 * pp) * 2 + pq * 128;
    LAS unsigned char *ub = S + LD_U + c * U_CH + 512 + 32 * a15 - 32 * (q >> 1) + 16 * (q & 1) - pq * 128;
#define RD_A(i) tr8(ka + (i) * 512 + pi * 64, ka + (i) * 512 + pi * 64 - 8)
#define RD_B(i) (*(LAS v4u *)(ub + (i) * 512 - pi * 64))
#pragma unroll
    for (int hf = 0; hf < 2; hf++) {
      LAS unsigned char *ex = S + LD_EX + hf * 32768 + c * 16384;
      f32x4 acc[4];
#pragma unroll
      for (int i = 0; i < 4; i++) acc[i] = f32x4{0.f, 0.f, 0.f, 0.f};
#pragma unroll
      for (int pi = 0; pi < 2; pi++) {
        v4u A[8], B[8];
        if (hf == 0) {
          A[0] = RD_A(0); B[0] = RD_B(0); B[1] = RD_B(1); B[2] = RD_B(2); B[3] = RD_B(3);
          A[1] = RD_A(1); A[2] = RD_A(2); A[3] = RD_A(3);
        } else {
          A[0] = RD_A(0); B[4] = RD_B(4); B[5] = RD_B(5); B[6] = RD_B(6); B[7] = RD_B(7);
          A[1] = RD_A(1); B[3] = RD_B(3); A[2] = RD_A(2); B[2] = RD_B(2); A[3] = RD_A(3); B[1] = RD_B(1);
          A[4] = RD_A(4); B[0] = RD_B(0); A[5] = RD_A(5); A[6] = RD_A(6); A[7] = RD_A(7);
        }
        PIN();
#pragma unroll
        for (int qq = 0; qq < 8; qq++) {
#pragma unroll
          for (int tl = 0; tl < 4; tl++)
            if (qq <= 4 * hf + tl) acc[tl] = mfma16h(A[qq], B[4 * hf + tl - qq], acc[tl]);
          PIN();
          if (hf == 1 && qq < 7) LDW1(7 * pi + qq);
        }
      }
      STAMP(17 + 2 * hf);
#pragma unroll
      for (int tl = 0; tl < 4; tl++) *(LAS f32x4 *)(ex + (pq * 4 + tl) * 1024 + lane16) = acc[tl];
      if (hf == 0) {
#pragma unroll 1
        for (unsigned sp = 0; sp < SPIN_CAP; sp++) {
          if (__all(wr == tag)) break;
          __builtin_amdgcn_s_sleep(1);
          wr = __builtin_amdgcn_raw_buffer_load_b32(rs, fo, 0, SC1);
        }
        __builtin_amdgcn_fence(__ATOMIC_ACQUIRE, "wavefront");
      } else {
        asm volatile("s_waitcnt vmcnt(14) ; PUBWAIT" ::: "memory");
      }
      BAR();
      if (hf == 0 && b == 0 && tid == 0) st4(rs, WS_EPOCH, tag);
      if (hf == 1 && tid < 8) __hip_atomic_fetch_add((gu32 *)(p.ws + WS_CNT) + (0 * 128 + tid * 16 + (m & 15)) * 64, 1u, RLX_AGENT);
      const int t = 4 * hf + pq;
      const int l0 = 256 * t + 16 * a15 + 4 * q;
      const f32x4 y0 = *(LAS f32x4 *)(ex + (0 * 4 + pq) * 1024 + lane16), y1 = *(LAS f32x4 *)(ex + (1 * 4 + pq) * 1024 + lane16);
      const f32x4 y2 = *(LAS f32x4 *)(ex + (2 * 4 + pq) * 1024 + lane16), y3 = *(LAS f32x4 *)(ex + (3 * 4 + pq) * 1024 + lane16);
      const v2u hh = *(LAS v2u *)(S + LD_U + c * U_CH + (256 + l0) * 2);
      const v2u xx = *(LAS v2u *)(S + LD_XS + c * 4096 + l0 * 2);
      const f32x4 y = (y0 + y1) + (y2 + y3);
      const h2 ha = ash2(hh.x), hb = ash2(hh.y);
      const float o0 = bflo(xx.x) + A1 * y[0] + A2 * (float)ha.x;
      const float o1 = bfhi(xx.x) + A1 * y[1] + A2 * (float)ha.y;
      const float o2 = bflo(xx.y) + A1 * y[2] + A2 * (float)hb.x;
      const float o3 = bfhi(xx.y) + A1 * y[3] + A2 * (float)hb.y;
      const u32 X0 = pkbf(o0, o1), X1 = pkbf(o2, o3);
      const u32 Y0 = pkbf(bflo(X0) * c2, bfhi(X0) * c2), Y1 = pkbf(bflo(X1) * c2, bfhi(X1) * c2);
      const auto r0 = __builtin_amdgcn_permlane16_swap(X0, Y0, false, false);
      const auto r1 = __builtin_amdgcn_permlane16_swap(X1, Y1, false, false);
      const int l8 = 256 * t + 16 * a15 + 8 * (q >> 1);
      st16(rs, ((q & 1) ? WS_X1S : WS_X1T) + ((l8 >> 6) * D + dch) * 128 + (l8 & 63) * 2, v4u{r0[0], r1[0], r0[1], r1[1]});
      PIN();
      STAMP(18 + 4 * hf);
    }
#pragma unroll
    for (int i = 14; i < 28; i++) LDW1(i);
    asm volatile("s_waitcnt vmcnt(14) ; PUBWAIT" ::: "memory");
    BAR();
    if (tid < 8) __hip_atomic_fetch_add((gu32 *)(p.ws + WS_CNT) + (1 * 128 + tid * 16 + (m & 15)) * 64, 1u, RLX_AGENT);
#undef RD_A
#undef RD_B
  }
  STAMP(7);

  if (w == 0) poll_counters(p, 0 * 128 + g * 16, 16, 14u, lane);
  BAR();
  STAMP(8);
  v4u xs[7];
  const int xso = tid * 16;
  const int xlo = (tid >> 3) * 128 + (((tid & 7) ^ (((tid >> 4) & 1) << 2)) * 16);
#pragma unroll
  for (int i = 0; i < 7; i++) xs[i] = ld16s(rs, xso, WS_X1S + tt * XB_SZ + i * 8192);
  PIN();
#pragma unroll
  for (int i = 0; i < 7; i++) *(LAS v4u *)(S + LD_XB + xlo + i * 8192) = xs[i];
  BAR();
  STAMP(9);
  v4u wd[28];
  v2u sg2[2], csg_ = {0, 0};
  u32 warm = 0;
  const int dtile = r % 7, t2 = 4 * g + r / 7;
  const int wdo = WS_WDF + (2 * dtile * 112 + 14 * w) * 1024;
  const int cso = WS_CSB + (phi * 64 + lane) * 8;
  {
    const int G = lane >> 4, e = (lane >> 2) & 3, pp = lane & 3, h = lane >> 5;
    const int sw = ((e >> 1) & 1) * 64;
    LAS unsigned char *bx0 = S + LD_XB + (8 * h + e) * 128 + ((0 + 32 * (G & 1) + 8 * pp) ^ sw);
    LAS unsigned char *bx1 = S + LD_XB + (8 * h + e) * 128 + ((64 + 32 * (G & 1) + 8 * pp) ^ sw);
#pragma unroll
    for (int hf = 0; hf < 2; hf++) {
      f32x16 acc[2];
#pragma unroll
      for (int j = 0; j < 2; j++)
#pragma unroll
        for (int i = 0; i < 16; i++) acc[j][i] = 0.f;
      v4u fb0[3], fb1[3];
      LAS unsigned char *bh0 = bx0 + hf * XB_SZ, *bh1 = bx1 + hf * XB_SZ;
      asm volatile("" : "+v"(bh0), "+v"(bh1));
#define RD_X(s) do { const int o_ = (s) * 2048; fb0[(s) % 3] = tr8(bh0 + o_, bh0 + o_ + 512); fb1[(s) % 3] = tr8(bh1 + o_, bh1 + o_ + 512); } while (0)
      RD_X(0); RD_X(1);
      const int sto = WS_STAT2 + (64 * (16 * hf + tt) + (lane & 31)) * 8;
#pragma unroll
      for (int s = 0; s < 28; s++) {
        if (s + 2 < 28) RD_X(s + 2);
        if (hf == 0) {
          if (s == 2) {
            if (w == 0) poll_counters(p, 1 * 128 + g * 16, 16, 14u, lane);
            BAR();
          }
          if (s >= 2 && s < 9) xs[s - 2] = ld16s(rs, xso, WS_X1S + (16 + tt) * XB_SZ + (s - 2) * 8192);
          if (s >= 16 && s < 23) *(LAS v4u *)(S + LD_XB + XB_SZ + xlo + (s - 16) * 8192) = xs[s - 16];
          if (s == 14) csg_ = ld8(rs, cso);
          if (s == 10) warm = __builtin_amdgcn_raw_buffer_load_b32(rs, (tid < 448 ? tid : 447) * 128,
                                  WS_WDF + (2 * dtile + ((r / 7) >> 1)) * 112 * 1024 + ((r / 7) & 1) * 57344, SC1);
        } else {
          if (s == 12) {
            asm volatile("s_waitcnt vmcnt(12) ; PUBWAIT" ::: "memory");
            BAR();
            if (tid == 0) __hip_atomic_fetch_add((gu32 *)(p.ws + WS_CNT) + (256 + tt) * 64, 1u, RLX_AGENT);
          }
          wd[s] = ld16s(rs, lane16, wdo + ((s / 14) * 112 + s % 14) * 1024);
        }
        if (s == 20) { sg2[0] = ld8(rs, sto); sg2[1] = ld8(rs, sto + 256); }
        PIN();
        acc[0] = mfma32b(w1[s], fb0[s % 3], acc[0]);
        acc[1] = mfma32b(w1[s], fb1[s % 3], acc[1]);
        PIN();
        if (s == 3) STAMP(23 + 3 * hf);
        if (s == 27) STAMP(24 + 3 * hf);
      }
#undef RD_X
      if (hf == 0) {
        const u32 cs = finish_granule(rs, cso, tag, csg_);
        *(LAS u32 *)(S + LD_CSL + (w * 64 + lane) * 4) = cs;
        asm volatile("" ::: "memory");
      } else {
        if (w == 0) poll_counters(p, 256 + (t2 >> 1), 1, 14u, lane);
      }
      const u32 sv0 = finish_granule(rs, sto, tag, sg2[0]), sv1 = finish_granule(rs, sto + 256, tag, sg2[1]);
      const int T = 16 * hf + tt;
      STAMP(25 + 3 * hf);
#pragma unroll
      for (int j = 0; j < 2; j++) {
        const u32 sv = j ? sv1 : sv0;
        const float mu = bflo(sv), rsd = __builtin_amdgcn_rcpf(bfhi(sv));
        u32 hp[4];
#pragma unroll
        for (int gq = 0; gq < 2; gq++) {
          LAS unsigned char *cl = S + LD_CSL + w * 256 + (8 * gq + 4 * h) * 4;
          const f32x4 csg = *(LAS f32x4 *)(cl), csu = *(LAS f32x4 *)(cl + 64);
          const f32x4 bg = *(LAS f32x4 *)(cl + 128), bu = *(LAS f32x4 *)(cl + 192);
          float hv[4];
#pragma unroll
          for (int i = 0; i < 4; i++) {
            const float gg = rsd * (acc[j][4 * gq + i] - mu * csg[i]) + bg[i];
            const float uu = rsd * (acc[j][8 + 4 * gq + i] - mu * csu[i]) + bu[i];
            const u32 gu = pkbf(gg, uu);
            hv[i] = bflo(gu) * bfhi(gu);
          }
          hp[2 * gq] = pkbf(hv[0], hv[1]);
          hp[2 * gq + 1] = pkbf(hv[2], hv[3]);
        }
        st16s(rs, lane16, WS_HF + ((2 * T + j) * 112 + phi) * 1024, v4u{hp[0], hp[1], hp[2], hp[3]});
      }
      PIN();
      if (hf == 0) BAR();
      STAMP(10 + hf);
    }
  }

  asm volatile("" ::"v"(warm));
  BAR();
  v4u gbg[2];
#pragma unroll
  for (int hf = 0; hf < 2; hf++) {
    const int tau = 32 * hf + t2;
    if (hf == 1) {
      if (w == 0) poll_counters(p, 256 + 16 + (t2 >> 1), 1, 14u, lane);
      BAR();
    }
    STAMP(12 + 2 * hf);
    v4u hb[14];
#pragma unroll
    for (int k = 0; k < 14; k++) hb[k] = ld16s(rs, lane16, WS_HF + (tau * 112 + 14 * w + k) * 1024);
    const v4u res = ld16(rs, WS_X1T + ((tau >> 1) * D + 64 * dtile + ((tid & 255) >> 2)) * 128 + (tau & 1) * 64 + (tid & 3) * 16);
    if (hf == 0) {
      gbg[0] = ld16(rs, WS_MODG + (5 * D + 64 * dtile + 4 * (tid & 15)) * 8);
      gbg[1] = ld16(rs, WS_MODG + (5 * D + 64 * dtile + 4 * (tid & 15) + 2) * 8);
      asm volatile("s_waitcnt vmcnt(17) ; PUBWAIT" ::: "memory");
      BAR();
      if (tid == 0) __hip_atomic_fetch_add((gu32 *)(p.ws + WS_CNT) + (256 + 16 + tt) * 64, 1u, RLX_AGENT);
    }
    PIN();
    f32x16 acc[2];
#pragma unroll
    for (int j = 0; j < 2; j++)
#pragma unroll
      for (int i = 0; i < 16; i++) acc[j][i] = 0.f;
#pragma unroll
    for (int k = 0; k < 14; k++) {
      acc[0] = mfma32b(wd[k], hb[k], acc[0]);
      acc[1] = mfma32b(wd[14 + k], hb[k], acc[1]);
    }
    {
      const int col = lane & 31, h = lane >> 5;
      LAS unsigned char *rd = S + LD_RED + w * RED_W + col * RED_ROW + h * 16;
#pragma unroll
      for (int dl = 0; dl < 2; dl++)
#pragma unroll
        for (int gq = 0; gq < 4; gq++)
          *(LAS f32x4 *)(rd + dl * 128 + gq * 32) =
              f32x4{acc[dl][4 * gq], acc[dl][4 * gq + 1], acc[dl][4 * gq + 2], acc[dl][4 * gq + 3]};
      if (tid < 256) *(LAS v4u *)(S + LD_RES + (tid >> 2) * 64 + (tid & 3) * 16) = res;
    }
    BAR();
    {
      const int tok = tid >> 4, dq = tid & 15;
      f32x4 part[8];
#pragma unroll
      for (int ww = 0; ww < 8; ww++) part[ww] = *(LAS f32x4 *)(S + LD_RED + ww * RED_W + tok * RED_ROW + dq * 16);
      LAS u16 *rp = (LAS u16 *)(S + LD_RES) + (4 * dq) * 32 + tok;
      const u32 r0 = rp[0], r1 = rp[32], r2 = rp[64], r3 = rp[96];
      const f32x4 sum = ((part[0] + part[1]) + (part[2] + part[3])) + ((part[4] + part[5]) + (part[6] + part[7]));
      const u32 b01 = pkbf(sum[0], sum[1]), b23 = pkbf(sum[2], sum[3]);
      const u32 t01 = pkbf(asf(gbg[0].x) * bflo(b01), asf(gbg[0].z) * bfhi(b01));
      const u32 t23 = pkbf(asf(gbg[1].x) * bflo(b23), asf(gbg[1].z) * bfhi(b23));
      const float o0 = bf2f(r0) + bflo(t01), o1 = bf2f(r1) + bfhi(t01);
      const float o2 = bf2f(r2) + bflo(t23), o3 = bf2f(r3) + bfhi(t23);
      const int l = 32 * tau + tok;
      *(GAS v2u *)(p.out + l * D + 64 * dtile + 4 * dq) = v2u{pkbf(o0, o1), pkbf(o2, o3)};
    }
    STAMP(13 + 2 * hf);
    if (hf == 0) BAR();
  }
}

__device__ __forceinline__ void side_role(const Params &p, LAS unsigned char *S, const rsrc_t rs, const int tid,
                                          const int lane, const int w, const int b, const u32 epoch) {
  const int g = b & 7, r = b >> 3, sidx = 4 * g + r - 28;
  const int sc = sidx < 28 ? sidx : 27;
  const int lane16 = lane * 16;
  const int tau = 16 * g + (r >> 1);
  const v4u x0 = *(const GAS v4u *)(p.x + (16 * tau + 2 * w) * D + (lane < 56 ? lane : 55) * 8);
  const v4u x1 = *(const GAS v4u *)(p.x + (16 * tau + 2 * w + 1) * D + (lane < 56 ? lane : 55) * 8);
  const u32 tag = __builtin_amdgcn_readfirstlane(epoch) + 1u;
  if (tid == 0) st4(rs, WS_WRDY + (224 + sidx) * 128, tag);
  if ((r & 1) == 0) {
    ln1_row(S, x0, 2 * w, lane);
    ln1_row(S, x1, 2 * w + 1, lane);
    STAMP(1);
    BAR();
    publish_xt(S, rs, lane, w, tau, tag);
  }
  STAMP(3);
  {
    const int dd = tid < D ? tid : 0;
    const int o1 = WS_WRDY + (tid < 224 ? tid : 0) * 128, o2 = WS_MODG + (4 * D + dd) * 8, o3 = WS_MODG + (3 * D + dd) * 8;
    const v2u g2 = ld8(rs, o2), g3 = ld8(rs, o3);
    const u32 vcb = finish_granule(rs, o2, tag, g2), vsb = finish_granule(rs, o3, tag, g3);
    if (tid < D) {
      ((LAS u16 *)(S + LD_CV))[tid] = (u16)(pkbf(1.0f + asf(vcb), 0.f) & 0xFFFFu);
      ((LAS u16 *)(S + LD_CV))[D + tid] = (u16)(vsb >> 16);
    }
#pragma unroll 1
    for (unsigned sp = 0; sp < SPIN_CAP; sp++) {
      const u32 wr = __builtin_amdgcn_raw_buffer_load_b32(rs, o1, 0, SC1);
      if (__all(wr == tag)) break;
      __builtin_amdgcn_s_sleep(2);
    }
    __builtin_amdgcn_fence(__ATOMIC_ACQUIRE, "wavefront");
  }
  BAR();
  STAMP(5);
  {
    const int ph = 4 * sc + (w >> 1), s0 = 14 * (w & 1);
    v4u wf[14];
#pragma unroll
    for (int k = 0; k < 14; k++) wf[k] = ld16s(rs, lane16, WS_W1F + (ph * 28 + s0 + k) * 1024);
    PIN();
    f32x16 acc;
#pragma unroll
    for (int i = 0; i < 16; i++) acc[i] = 0.f;
    LAS unsigned char *cvp = S + LD_CV + ((lane & 31) == 1 ? D * 2 : 0) + (lane >> 5) * 16;
#pragma unroll
    for (int k = 0; k < 14; k++) acc = mfma32b(wf[k], *(LAS v4u *)(cvp + (s0 + k) * 32), acc);
    LAS float *pr = (LAS float *)(S) + w * 64;
    if ((lane & 31) < 2) {
#pragma unroll
      for (int i = 0; i < 16; i++) pr[(lane & 31) * 32 + (i & 3) + 8 * (i >> 2) + 4 * (lane >> 5)] = acc[i];
    }
  }
  BAR();
  if (sidx < 28 && tid < 256) {
    const int pl = tid >> 6, e = tid & 63;
    const float v = ((LAS float *)S)[(2 * pl) * 64 + e] + ((LAS float *)S)[(2 * pl + 1) * 64 + e];
    st8(rs, WS_CSB + ((4 * sidx + pl) * 64 + e) * 8, v2u{__builtin_bit_cast(u32, v), tag});
  }
  STAMP(6);
#pragma unroll 1
  for (int hf = 0; hf < 2; hf++) {
    if (w == 0) poll_counters(p, hf * 128 + g * 16, 16, 14u, lane);
    BAR();
    const int tau = 32 * hf + sidx;
    const int tp = lane & 15, rgp = (lane >> 4) + 4 * w;
    u32 xv[14];
#pragma unroll
    for (int i = 0; i < 14; i++)
      xv[i] = __builtin_amdgcn_raw_buffer_load_b32(rs, rgp * 128 + tp * 4, WS_X1T + ((tau >> 1) * D + 32 * i) * 128 + (tau & 1) * 64, SC1);
    PIN();
    float s0 = 0.f, s1 = 0.f, q0 = 0.f, q1 = 0.f;
#pragma unroll
    for (int i = 0; i < 14; i++) {
      const float a = bflo(xv[i]), c = bfhi(xv[i]);
      s0 += a; s1 += c; q0 += a * a; q1 += c * c;
    }
    LAS f32x4 *pr = (LAS f32x4 *)(S + 4096);
    pr[rgp * 16 + tp] = f32x4{s0, s1, q0, q1};
    BAR();
    if (tid < 128) {
      const int tok = tid >> 2, part = tid & 3;
      float s = 0.f, q = 0.f;
#pragma unroll
      for (int i = 0; i < 8; i++) {
        const f32x4 v = pr[(part * 8 + i) * 16 + (tok >> 1)];
        s += (tok & 1) ? v[1] : v[0];
        q += (tok & 1) ? v[3] : v[2];
      }
      s += dpp0<0xB1, 0xF>(s); q += dpp0<0xB1, 0xF>(q);
      s += dpp0<0x4E, 0xF>(s); q += dpp0<0x4E, 0xF>(q);
      const float mean = s * (1.0f / D);
      const float var = rbf(fmaxf(q * (1.0f / D) - mean * mean, 0.f));
      const float sd = rbf(__builtin_amdgcn_sqrtf(rbf(var + 1e-5f)));
      if (part == 0) st8(rs, WS_STAT2 + (32 * tau + tok) * 8, v2u{pkbf(mean, sd), tag});
    }
    STAMP(8 + hf);
  }
}

__global__ void __launch_bounds__(NTHR, 2) k_main(Params p) {
  extern __shared__ __attribute__((aligned(16))) unsigned char smem[];
  LAS unsigned char *S = (LAS unsigned char *)smem;
  const int tid = threadIdx.x, lane = tid & 63, b = blockIdx.x;
  const int w = __builtin_amdgcn_readfirstlane(tid >> 6);
  const rsrc_t rs = __builtin_amdgcn_make_buffer_rsrc((void *)p.ws, 0, WS_END, 0x00020000);
  u32 pf;
  {
    unsigned long long pc;
    asm volatile("s_getpc_b64 %0" : "=s"(pc));
    pf = *(const GAS u32 *)((pc & ~127ull) + (unsigned)(tid < PF_LINES ? tid : 0) * 128u);
  }
  const u32 epoch = ld8(rs, WS_EPOCH).x;
  STAMP(0);
  if ((b >> 3) < 28) main_role(p, S, rs, tid, lane, w, b, epoch);
  else side_role(p, S, rs, tid, lane, w, b, epoch);
  asm volatile("" ::"v"(pf));
}

}

extern "C" void kernel_launch(void *const *d_in, const int *in_sizes, int n_in, void *d_out, int out_size, void *d_ws,
                              size_t ws_size, hipStream_t stream) {
  using namespace mk;
  Params p;
  p.x = (const u16 *)d_in[0];
  p.Are = (const u16 *)d_in[1]; p.Aim = (const u16 *)d_in[2];
  p.Bre = (const u16 *)d_in[3]; p.Bim = (const u16 *)d_in[4];
  p.Cre = (const u16 *)d_in[5]; p.Cim = (const u16 *)d_in[6];
  p.logdt = (const u16 *)d_in[7]; p.Dsk = (const u16 *)d_in[8];
  p.wg = (const u16 *)d_in[10]; p.wu = (const u16 *)d_in[11]; p.wd = (const u16 *)d_in[12];
  p.cvec = (const u16 *)d_in[13]; p.wmod = (const u16 *)d_in[14];
  p.out = (u16 *)d_out;
  p.ws = (unsigned char *)d_ws;
  (void)hipFuncSetAttribute((const void *)k_main, hipFuncAttributeMaxDynamicSharedMemorySize, LDS_BYTES);
  k_main<<<NWG, NTHR, LDS_BYTES, stream>>>(p);
}
```

```cpp
#define MK_PROBE 1
#include <hip/hip_runtime.h>
#include <stdint.h>

namespace mk {

typedef unsigned short u16;
typedef unsigned u32;
typedef u32 v4u __attribute__((ext_vector_type(4)));
typedef u32 v2u __attribute__((ext_vector_type(2)));
typedef float f2 __attribute__((ext_vector_type(2)));
typedef float f32x4 __attribute__((ext_vector_type(4)));
typedef float f32x16 __attribute__((ext_vector_type(16)));
typedef _Float16 h2 __attribute__((ext_vector_type(2)));
typedef _Float16 h8 __attribute__((ext_vector_type(8)));
typedef __bf16 b2 __attribute__((ext_vector_type(2)));
typedef __bf16 b8 __attribute__((ext_vector_type(8)));
typedef short s4 __attribute__((ext_vector_type(4)));
#define LAS __attribute__((address_space(3)))
#define GAS __attribute__((address_space(1)))
typedef GAS u32 gu32;
#define RLX_AGENT __ATOMIC_RELAXED, __HIP_MEMORY_SCOPE_AGENT

constexpr int L = 2048, D = 448, NS = 256, F = 1792;
constexpr int NWG = 256, NTHR = 512;
constexpr int SC1 = 16;
constexpr int PF_LINES = 190;

constexpr int WS_EPOCH = 0;
constexpr int WS_CNT = 4096;
constexpr int NCNT = 288;
constexpr int WS_STAT1 = WS_CNT + NCNT * 256;
constexpr int WS_MODG = WS_STAT1 + 2048 * 8;
constexpr int WS_CSB = WS_MODG + 2688 * 8;
constexpr int WS_STAT2 = WS_CSB + 112 * 64 * 8;
constexpr int WS_WRDY = WS_STAT2 + 2048 * 8;
constexpr int WS_XG = WS_WRDY + 256 * 16;
constexpr int WS_W1F = WS_XG + 128 * 224 * 128;
constexpr int WS_WDF = WS_W1F + 112 * 28 * 1024;
constexpr int WS_X1T = WS_WDF + 14 * 112 * 1024;
constexpr int WS_X1S = WS_X1T + 32 * 448 * 128;
constexpr int WS_HF = WS_X1S + 32 * 448 * 128;
constexpr int WS_DBG = WS_HF + 64 * 112 * 1024;
constexpr int WS_END = WS_DBG + (8 << 20);
static_assert(WS_STAT1 % 128 == 0 && WS_MODG % 128 == 0 && WS_CSB % 128 == 0 && WS_STAT2 % 128 == 0 && WS_XG % 128 == 0 && WS_W1F % 128 == 0, "align");

constexpr int PQ_ROW = 1056, PQ_CH = 57 * PQ_ROW;
constexpr int LD_PQ = 0;
constexpr int LD_KC = 0;
constexpr int KC_CP = 4160, KC_CH = 4 * KC_CP;
constexpr int LD_EX = 33280;
constexpr int LD_XB = 0;
constexpr int XB_SZ = 448 * 128;
constexpr int LD_RED = 0;
constexpr int RED_ROW = 272, RED_W = 32 * RED_ROW;
constexpr int LD_RES = 8 * RED_W;
constexpr int LD_KD = 120384;
constexpr int KD_CH = 784 * 2;
constexpr int LD_U = LD_KD + 2 * KD_CH;
constexpr int U_CH = 2304 * 2;
constexpr int LD_XS = LD_U + 2 * U_CH;
constexpr int LD_ST1 = 140928;
constexpr int LD_XROW = 120384;
constexpr int LD_WSTG = LD_XROW + 16128;
constexpr int WSTG_ROW = 464, WSTG_M = 16 * WSTG_ROW;
constexpr int LD_SMALL = LD_WSTG + 3 * WSTG_M;
constexpr int LD_STAT = LD_SMALL;
constexpr int LD_WMAX = LD_SMALL + 64;
constexpr int LD_CSL = LD_SMALL + 96;
constexpr int LD_STAMP = LD_CSL + 2048;
constexpr int LD_CV = LD_STAMP + 256;
constexpr int LD_END = LD_CV + 2 * 448 * 2;
constexpr int LDS_BYTES = 160 * 1024;
static_assert(LD_XS + 8192 <= LD_ST1 && LD_ST1 + 16384 <= LD_SMALL && LD_XS + 8192 <= LD_SMALL && LD_END <= LDS_BYTES && LD_CSL % 16 == 0 && LD_CV % 16 == 0, "lds");
static_assert(LD_RES + 4096 <= LD_KD, "lds red");

struct Params {
  const u16 *x, *Are, *Aim, *Bre, *Bim, *Cre, *Cim, *logdt, *Dsk, *wg, *wu, *wd, *cvec, *wmod;
  u16 *out;
  unsigned char *ws;
};

struct KTab { u16 v[256][16]; };
constexpr int bf16_round_int(int j) {
  if (j < 256) return j;
  int sh = 0; int t = j; while (t >= 256) { t >>= 1; sh++; }
  int q = j >> sh, rem = j & ((1 << sh) - 1), half = 1 << (sh - 1);
  if (rem > half || (rem == half && (q & 1))) q++;
  return q << sh;
}
constexpr int kd_index(int j) {
  if (j < 0) return 768;
  int l = bf16_round_int(j);
  if (l >= 2048) return 40 * 16;
  int k = l < 256 ? 0 : (l < 512 ? 1 : (l < 1024 ? 2 : 3));
  int base = k ? (128 << k) : 0;
  int rem = l - base;
  int a = rem >> 7, b = (rem >> 3) & 15, c = (rem & 7) >> k;
  int row = (k ? 8 + 8 * k : 0) + a * (8 >> k) + c;
  return row * 16 + b;
}
constexpr KTab make_ktab() {
  KTab t{};
  for (int i = 0; i < 256; i++)
    for (int j = 0; j < 16; j++) t.v[i][j] = (u16)(2 * (j < 11 ? kd_index(8 * i - 3 + j) : 768));
  return t;
}
__device__ const KTab g_ktab = make_ktab();

__device__ __forceinline__ float asf(u32 v) { return __builtin_bit_cast(float, v); }
__device__ __forceinline__ h2 ash2(u32 v) { return __builtin_bit_cast(h2, v); }
__device__ __forceinline__ float bf2f(u32 v) { return __builtin_bit_cast(float, v << 16); }
__device__ __forceinline__ float bflo(u32 v) { return __builtin_bit_cast(float, v << 16); }
__device__ __forceinline__ float bfhi(u32 v) { return __builtin_bit_cast(float, v & 0xFFFF0000u); }
__device__ __forceinline__ u32 pkbf(float a, float b) {
  return __builtin_bit_cast(u32, __builtin_convertvector(f2{a, b}, b2));
}
__device__ __forceinline__ u32 pkh(float a, float b) {
  return __builtin_bit_cast(u32, __builtin_convertvector(f2{a, b}, h2));
}
__device__ __forceinline__ float rbf(float a) { return bflo(pkbf(a, a)); }
__device__ __forceinline__ f2 cmul(f2 a, f2 b) {
  f2 r;
  asm("v_pk_mul_f32 %0, %1, %2 op_sel_hi:[0,1]\n\t"
      "v_pk_fma_f32 %0, %1, %2, %0 op_sel:[1,1,0] op_sel_hi:[1,0,1] neg_lo:[1,0,0]"
      : "=&v"(r) : "v"(a), "v"(b));
  return r;
}
__device__ __forceinline__ float dot2bf(u32 a, u32 b, float c) {
  return __builtin_amdgcn_fdot2_f32_bf16(__builtin_bit_cast(b2, a), __builtin_bit_cast(b2, b), c, false);
}
template <class T> __device__ __forceinline__ T gld(const void *base, u32 off) { return *(const GAS T *)((const GAS char *)base + off); }
template <int CTRL, int RM> __device__ __forceinline__ float dpp0(float v) {
  return __builtin_bit_cast(float, __builtin_amdgcn_update_dpp(0, __builtin_bit_cast(int, v), CTRL, RM, 0xF, true));
}
__device__ __forceinline__ float wave_sum(float v) {
  v += dpp0<0x111, 0xF>(v);
  v += dpp0<0x112, 0xF>(v);
  v += dpp0<0x114, 0xF>(v);
  v += dpp0<0x118, 0xF>(v);
  v += dpp0<0x142, 0xA>(v);
  v += dpp0<0x143, 0xC>(v);
  return __builtin_bit_cast(float, __builtin_amdgcn_readlane(__builtin_bit_cast(int, v), 63));
}
__device__ __forceinline__ float wave_max_nonneg(float v) {
  v = fmaxf(v, dpp0<0x111, 0xF>(v));
  v = fmaxf(v, dpp0<0x112, 0xF>(v));
  v = fmaxf(v, dpp0<0x114, 0xF>(v));
  v = fmaxf(v, dpp0<0x118, 0xF>(v));
  v = fmaxf(v, dpp0<0x142, 0xA>(v));
  v = fmaxf(v, dpp0<0x143, 0xC>(v));
  return __builtin_bit_cast(float, __builtin_amdgcn_readlane(__builtin_bit_cast(int, v), 63));
}
__device__ __forceinline__ v2u trread(LAS unsigned char *p) {
  return __builtin_bit_cast(v2u, __builtin_amdgcn_ds_read_tr16_b64_v4i16((LAS s4 *)p));
}
__device__ __forceinline__ v4u tr8(LAS unsigned char *p0, LAS unsigned char *p1) {
  v2u a = trread(p0), b = trread(p1);
  return v4u{a.x, a.y, b.x, b.y};
}
__device__ __forceinline__ f32x4 mfma16h(v4u a, v4u b, f32x4 c) {
  return __builtin_amdgcn_mfma_f32_16x16x32_f16(__builtin_bit_cast(h8, a), __builtin_bit_cast(h8, b), c, 0, 0, 0);
}
__device__ __forceinline__ f32x16 mfma32b(v4u a, v4u b, f32x16 c) {
  return __builtin_amdgcn_mfma_f32_32x32x16_bf16(__builtin_bit_cast(b8, a), __builtin_bit_cast(b8, b), c, 0, 0, 0);
}
#define BAR() asm volatile("s_waitcnt lgkmcnt(0)\n\ts_barrier" ::: "memory")
#define DRAIN() asm volatile("s_waitcnt vmcnt(0)" ::: "memory")
#define PIN() do { asm volatile("" ::: "memory"); __builtin_amdgcn_sched_barrier(0); } while (0)
typedef __amdgpu_buffer_rsrc_t rsrc_t;
__device__ __forceinline__ v4u ld16(rsrc_t rs, int off) { return __builtin_amdgcn_raw_buffer_load_b128(rs, off, 0, SC1); }
__device__ __forceinline__ v4u ld16s(rsrc_t rs, int voff, int soff) { return __builtin_amdgcn_raw_buffer_load_b128(rs, voff, soff, SC1); }
__device__ __forceinline__ void st16s(rsrc_t rs, int voff, int soff, v4u v) { __builtin_amdgcn_raw_buffer_store_b128(v, rs, voff + soff, 0, SC1); }
__device__ __forceinline__ v2u ld8(rsrc_t rs, int off) { return __builtin_amdgcn_raw_buffer_load_b64(rs, off, 0, SC1); }
__device__ __forceinline__ void st16(rsrc_t rs, int off, v4u v) { __builtin_amdgcn_raw_buffer_store_b128(v, rs, off, 0, SC1); }
__device__ __forceinline__ void st8(rsrc_t rs, int off, v2u v) { __builtin_amdgcn_raw_buffer_store_b64(v, rs, off, 0, SC1); }
__device__ __forceinline__ void st4(rsrc_t rs, int off, u32 v) { __builtin_amdgcn_raw_buffer_store_b32(v, rs, off, 0, SC1); }

constexpr unsigned SPIN_CAP = 1u << 14;

#define MK_PWG 0
#define STAMP(i) do { if ((i) == MK_PROBE && b == MK_PWG && w == 0) probe_t = __builtin_amdgcn_s_memrealtime(); } while (0)

__device__ __forceinline__ u32 peek_counters(const Params &p, int idx0, int ns, int lane) {
  return __hip_atomic_load((gu32 *)(p.ws + WS_CNT) + (idx0 + (lane < ns ? lane : 0)) * 64, RLX_AGENT);
}
__device__ __forceinline__ void finish_counters(const Params &p, int idx0, int ns, u32 want, int lane, u32 v) {
#pragma unroll 1
  for (unsigned sp = 0; sp < SPIN_CAP; sp++) {
    if (__all(v >= want)) break;
    __builtin_amdgcn_s_sleep(1);
    v = peek_counters(p, idx0, ns, lane);
  }
  __builtin_amdgcn_fence(__ATOMIC_ACQUIRE, "wavefront");
}
__device__ __forceinline__ void poll_counters(const Params &p, int idx0, int ns, u32 want, int lane) {
  finish_counters(p, idx0, ns, want, lane, peek_counters(p, idx0, ns, lane));
}
__device__ __forceinline__ u32 finish_granule(rsrc_t rs, int off, u32 tag, v2u g) {
#pragma unroll 1
  for (unsigned sp = 0; sp < SPIN_CAP; sp++) {
    if (__all(g.y == tag)) break;
    __builtin_amdgcn_s_sleep(1);
    g = ld8(rs, off);
  }
  __builtin_amdgcn_fence(__ATOMIC_ACQUIRE, "wavefront");
  return g.x;
}

template <int ROW> __device__ __forceinline__ void pqw4(u32 m0v, f2 a, f2 b, f2 c, f2 d) {
  const u32 h0 = pkh(a.x, a.y), h1 = pkh(b.x, b.y), h2_ = pkh(c.x, c.y), h3 = pkh(d.x, d.y);
  asm volatile("s_mov_b32 m0, %4\n\ts_nop 0\n\tds_write_addtid_b32 %0 offset:%c5\n\tds_write_addtid_b32 %1 offset:%c6\n\t"
               "ds_write_addtid_b32 %2 offset:%c7\n\tds_write_addtid_b32 %3 offset:%c8"
               ::"v"(h0), "v"(h1), "v"(h2_), "v"(h3), "s"(m0v), "i"(ROW * PQ_ROW), "i"((ROW + 1) * PQ_ROW),
               "i"((ROW + 2) * PQ_ROW), "i"((ROW + 3) * PQ_ROW) : "memory");
}
template <int ROW> __device__ __forceinline__ void pqw1(u32 m0v, f2 a) {
  const u32 h0 = pkh(a.x, a.y);
  asm volatile("s_mov_b32 m0, %1\n\ts_nop 0\n\tds_write_addtid_b32 %0 offset:%c2" ::"v"(h0), "s"(m0v), "i"(ROW * PQ_ROW) : "memory");
}
__device__ __forceinline__ f2 zpow(float al, float rv) {
  const float e = __builtin_amdgcn_exp2f(al);
  const float r = __builtin_amdgcn_fractf(rv);
  return f2{e * __builtin_amdgcn_cosf(r), e * __builtin_amdgcn_sinf(r)};
}
__device__ __forceinline__ float pow2b(int e) { return asf((u32)e << 23); }

__device__ __forceinline__ void ln1_rows(LAS unsigned char *S, v4u x0, v4u x1, int w, int lane) {
  constexpr u32 ONES = 0x3F803F80u;
  float v[4];
  v[0] = dot2bf(x0.w, ONES, dot2bf(x0.z, ONES, dot2bf(x0.y, ONES, dot2bf(x0.x, ONES, 0.f))));
  v[1] = dot2bf(x0.w, x0.w, dot2bf(x0.z, x0.z, dot2bf(x0.y, x0.y, dot2bf(x0.x, x0.x, 0.f))));
  v[2] = dot2bf(x1.w, ONES, dot2bf(x1.z, ONES, dot2bf(x1.y, ONES, dot2bf(x1.x, ONES, 0.f))));
  v[3] = dot2bf(x1.w, x1.w, dot2bf(x1.z, x1.z, dot2bf(x1.y, x1.y, dot2bf(x1.x, x1.x, 0.f))));
  if (lane < 56) {
    *(LAS v4u *)(S + LD_XROW + (2 * w) * 960 + lane * 16) = x0;
    *(LAS v4u *)(S + LD_XROW + (2 * w + 1) * 960 + lane * 16) = x1;
  }
#pragma unroll
  for (int i = 0; i < 4; i++) v[i] = lane < 56 ? v[i] : 0.f;
#pragma unroll
  for (int i = 0; i < 4; i++) v[i] += dpp0<0x111, 0xF>(v[i]);
#pragma unroll
  for (int i = 0; i < 4; i++) v[i] += dpp0<0x112, 0xF>(v[i]);
#pragma unroll
  for (int i = 0; i < 4; i++) v[i] += dpp0<0x114, 0xF>(v[i]);
#pragma unroll
  for (int i = 0; i < 4; i++) v[i] += dpp0<0x118, 0xF>(v[i]);
#pragma unroll
  for (int i = 0; i < 4; i++) v[i] += dpp0<0x142, 0xA>(v[i]);
#pragma unroll
  for (int i = 0; i < 4; i++) v[i] += dpp0<0x143, 0xC>(v[i]);
  const float s1 = asf(__builtin_amdgcn_readlane(__builtin_bit_cast(int, v[2]), 63));
  const float q1 = asf(__builtin_amdgcn_readlane(__builtin_bit_cast(int, v[3]), 63));
  const float s = lane == 62 ? s1 : v[0], q = lane == 62 ? q1 : v[1];
  const float mean = s * (1.0f / D);
  const float var = rbf(fmaxf(q * (1.0f / D) - mean * mean, 0.f));
  const float sd = rbf(__builtin_amdgcn_sqrtf(rbf(var + 1e-5f)));
  if (lane >= 62) *(LAS u32 *)(S + LD_STAT + (2 * w + 63 - lane) * 4) = pkbf(mean, sd);
}
__device__ __forceinline__ void publish_xt(LAS unsigned char *S, const rsrc_t rs, int lane, int w, int tau, u32 tag) {
  const int G = lane >> 4, e = (lane >> 2) & 3, pp = lane & 3;
  LAS unsigned char *ax = S + LD_XROW + (4 * G + e) * 960 + 4 * pp * 2 + w * 32;
  const int go = WS_XG + tau * (224 * 128) + w * 1024 + (lane & 15) * 64 + G * 16;
  const v2u v0 = trread(ax), v1 = trread(ax + 256), v2 = trread(ax + 512), v3 = trread(ax + (w < 4 ? 768 : 512));
  st16(rs, go, v4u{v0.x, tag, v0.y, tag});
  st16(rs, go + 8192, v4u{v1.x, tag, v1.y, tag});
  st16(rs, go + 16384, v4u{v2.x, tag, v2.y, tag});
  if (w < 4) st16(rs, go + 24576, v4u{v3.x, tag, v3.y, tag});
  if (w == 7 && lane < 16) st8(rs, WS_STAT1 + (16 * tau + lane) * 8, v2u{*(LAS u32 *)(S + LD_STAT + lane * 4), tag});
}

__device__ __forceinline__ void main_role(const Params &p, LAS unsigned char *S, const rsrc_t rs, const int tid,
                                          const int lane, const int w, const int b, const u32 epoch, unsigned long long &probe_t) {
  const int g = b & 7, r = b >> 3, m = 28 * g + r;
  const int cch = w >> 2, t8 = tid & 255, dch = 2 * m + cch;
  const int lane16 = lane * 16;
  const bool xprod = (r & 1) == 0;
  const int tau = 16 * g + (r >> 1), mi = 14 * g + (r >> 1);
  const u32 tm = tid < 504 ? tid : 503, mrow = (tm * 21846u) >> 16, mpc = tm - 3 * mrow;
  v4u rl[3];
  u32 cv[3];
  {
    const void *rb = xprod ? (const void *)p.x : (const void *)p.wmod;
    const u32 lo = xprod ? (u32)(lane < 56 ? lane : 55) * 16u : mrow * (12u * D) + mpc * 16u;
    const u32 u0 = xprod ? (u32)(16 * tau + 2 * w) * (2u * D) : (u32)mi * 48u;
    const u32 st = xprod ? 2u * D : 168u * 12u * D;
    const bool v2 = xprod || mrow < 112;
    rl[0] = gld<v4u>(rb, lo + u0);
    rl[1] = gld<v4u>(rb, lo + u0 + st);
    rl[2] = gld<v4u>(rb, lo + u0 + (xprod ? st : (v2 ? 2 * st : 0u)));
    cv[0] = gld<u16>(p.cvec, mrow * 2u);
    cv[1] = gld<u16>(p.cvec, mrow * 2u + 336u);
    cv[2] = gld<u16>(p.cvec, (mrow < 112 ? mrow : 111u) * 2u + 672u);
  }
  PIN();
  const u32 po = (u32)(dch * NS + t8) * 2u;
  const u32 pAre = gld<u16>(p.Are, po), pAim = gld<u16>(p.Aim, po), pBre = gld<u16>(p.Bre, po), pBim = gld<u16>(p.Bim, po);
  const u32 pCre = gld<u16>(p.Cre, po), pCim = gld<u16>(p.Cim, po);
  const u32 sLdt = *(const __attribute__((address_space(4))) u32 *)(p.logdt + 2 * m);
  const u32 sDsk = *(const __attribute__((address_space(4))) u32 *)(p.Dsk + 2 * m);
  const u32 pLdt = cch ? sLdt >> 16 : sLdt & 0xFFFFu, pDsk = cch ? sDsk >> 16 : sDsk & 0xFFFFu;
  const u32 tc = tid < 448 ? tid : 447, srow = (tc * 2341u) >> 16, scol = tc - 28 * srow;
  const v4u ws0 = gld<v4u>(p.wg, srow * (2u * F) + scol * 16u + (u32)(16 * (m >> 3) * F + 224 * (m & 7)) * 2u);
  const v4u ws1 = gld<v4u>(p.wu, srow * (2u * F) + scol * 16u + (u32)(16 * (m >> 3) * F + 224 * (m & 7)) * 2u);
  const v4u ws2 = gld<v4u>(p.wd, srow * (2u * D) + scol * 16u + (u32)(16 * (m >> 1) * D + 224 * (m & 1)) * 2u);
  PIN();
  asm volatile("" : "+v"(rl[0]), "+v"(rl[1]), "+v"(rl[2]));
  const u32 tag = __builtin_amdgcn_readfirstlane(epoch) + 1u;

  if (xprod) {
    ln1_rows(S, rl[0], rl[1], w, lane);
  } else {
    f2 a[4] = {f2{0.f, 0.f}, f2{0.f, 0.f}, f2{0.f, 0.f}, f2{0.f, 0.f}};
#pragma unroll
    for (int i = 0; i < 3; i++) {
      const float cx = bf2f(cv[i]);
      float sl = rbf(cx * __builtin_amdgcn_rcpf(1.0f + __builtin_amdgcn_exp2f(cx * -1.44269504088896f)));
      if (i == 2) sl = mrow < 112 ? sl : 0.f;
      const f2 s2 = {sl, sl};
      a[0] = __builtin_elementwise_fma(s2, f2{bflo(rl[i].x), bfhi(rl[i].x)}, a[0]);
      a[1] = __builtin_elementwise_fma(s2, f2{bflo(rl[i].y), bfhi(rl[i].y)}, a[1]);
      a[2] = __builtin_elementwise_fma(s2, f2{bflo(rl[i].z), bfhi(rl[i].z)}, a[2]);
      a[3] = __builtin_elementwise_fma(s2, f2{bflo(rl[i].w), bfhi(rl[i].w)}, a[3]);
    }
    if (tid < 504) {
      LAS f32x4 *mp = (LAS f32x4 *)(S + LD_XROW + tm * 32);
      mp[0] = f32x4{a[0].x, a[0].y, a[1].x, a[1].y}; mp[1] = f32x4{a[2].x, a[2].y, a[3].x, a[3].y};
    }
  }
  if (tid < 448) {
    LAS unsigned char *wp = S + LD_WSTG + srow * WSTG_ROW + scol * 16;
    *(LAS v4u *)(wp) = ws0;
    *(LAS v4u *)(wp + WSTG_M) = ws1;
    *(LAS v4u *)(wp + 2 * WSTG_M) = ws2;
  }
  asm volatile("" ::"v"(ws0), "v"(ws1), "v"(ws2), "v"(pAre), "v"(pAim), "v"(pBre), "v"(pBim), "v"(pCre), "v"(pCim));
  STAMP(1);
  BAR();
  if (xprod) {
    publish_xt(S, rs, lane, w, tau, tag);
  } else if (w < 6) {
    const int col = tid >> 4, part = tid & 15;
    LAS float *mp = (LAS float *)(S + LD_XROW) + part * 24 + col;
    float sm = part < 8 ? mp[160 * 24] : 0.f;
#pragma unroll
    for (int i = 0; i < 10; i++) sm += mp[i * 16 * 24];
    sm += dpp0<0x111, 0xF>(sm); sm += dpp0<0x112, 0xF>(sm); sm += dpp0<0x114, 0xF>(sm); sm += dpp0<0x118, 0xF>(sm);
    if (part == 15) st8(rs, WS_MODG + (24 * mi + col) * 8, v2u{__builtin_bit_cast(u32, rbf(sm)), tag});
  }
  PIN();
  STAMP(2);

  const float dt = rbf(__builtin_amdgcn_exp2f(bf2f(pLdt) * 1.44269504088896f));
  const float al = (dt * bf2f(pAre)) * 1.44269504088896f;
  const float rv = (dt * bf2f(pAim)) * 0.159154943091895f;
  const f2 Wn = cmul(f2{bf2f(pCre), -bf2f(pCim)}, f2{dt * bf2f(pBre), -dt * bf2f(pBim)});
  {
    const float mx = wave_max_nonneg(fmaxf(fabsf(Wn.x), fabsf(Wn.y)));
    if (lane == 0) *(LAS float *)(S + LD_WMAX + w * 4) = mx;
  }
  const u32 m0v = (u32)(LD_PQ + (w >> 2) * PQ_CH + (w & 3) * 256);
  const f2 p1 = zpow(al, rv), z8 = zpow(al * 8.f, rv * -8.f), Z1 = zpow(al * 128.f, rv * 128.f);
  const f2 p2 = cmul(p1, p1), p3 = cmul(p2, p1), p4 = cmul(p2, p2), p5 = cmul(p4, p1), p6 = cmul(p4, p2), p7 = cmul(p4, p3);
  pqw4<0>(m0v, f2{1.f, 0.f}, p1, p2, p3);
  pqw4<4>(m0v, p4, p5, p6, p7);
  pqw4<8>(m0v, Z1, cmul(Z1, p1), cmul(Z1, p2), cmul(Z1, p3));
  pqw4<12>(m0v, cmul(Z1, p4), cmul(Z1, p5), cmul(Z1, p6), cmul(Z1, p7));
  PIN();
  v4u wdfrag;
  {
    const int G = lane >> 4, e = (lane >> 2) & 3, pp = lane & 3, h = G >> 1;
    LAS unsigned char *a0 = S + LD_WSTG + (G & 1) * WSTG_M + (8 * h + e) * WSTG_ROW + (16 * w + 4 * pp) * 2;
    LAS unsigned char *a1 = a0 + (w < 6 ? 256 : 0);
    LAS unsigned char *a2 = S + LD_WSTG + 2 * WSTG_M + (4 * h + e) * WSTG_ROW + (32 * (w < 7 ? w : 6) + 16 * (G & 1) + 4 * pp) * 2;
    const v4u v0 = tr8(a0, a0 + 4 * WSTG_ROW), v1 = tr8(a1, a1 + 4 * WSTG_ROW);
    wdfrag = tr8(a2, a2 + 8 * WSTG_ROW);
    const int o1 = WS_W1F + ((14 * (m & 7) + w) * 28 + (m >> 3)) * 1024 + lane16;
    st16(rs, o1, v0);
    if (w < 6) st16(rs, o1 + 8 * 28 * 1024, v1);
    if (w == 7 && lane < 2 && m + 224 * lane < NCNT) st4(rs, WS_CNT + (m + 224 * lane) * 256, 0u);
  }
  PIN();
  STAMP(3);
  v4u xg[2], sg[2], mg[4], kt0;
  v2u kt1;
  int ebW;
  const int hch = (tid >> 2) & 1, l4 = 16 * (tid >> 3) + 4 * (tid & 3);
  const int xo = ((tid >> 3) * 224) * 128 + (tid & 7) * 16;
  const int xu = WS_XG + m * 128, mo = WS_MODG + 2 * m * 8;
#define LD_HANDOFF_A() do { \
    sg[0] = ld16s(rs, tid * 16, WS_STAT1); sg[1] = ld16s(rs, tid * 16, WS_STAT1 + 8192); \
    xg[0] = ld16s(rs, xo, xu); xg[1] = ld16s(rs, xo, xu + 64 * 224 * 128); \
    mg[0] = ld16s(rs, 0, mo + 0 * D * 8); mg[1] = ld16s(rs, 0, mo + 1 * D * 8); \
    mg[2] = ld16s(rs, 0, mo + 2 * D * 8); mg[3] = ld16s(rs, 0, mo + 4 * D * 8); } while (0)
  {
    const f2 Z2 = cmul(Z1, Z1), Z3 = cmul(Z2, Z1), Z4 = cmul(Z2, Z2);
    pqw4<16>(m0v, Z2, cmul(Z2, p2), cmul(Z2, p4), cmul(Z2, p6));
    pqw4<20>(m0v, Z3, cmul(Z3, p2), cmul(Z3, p4), cmul(Z3, p6));
    const f2 Z5 = cmul(Z4, Z1), Z6 = cmul(Z4, Z2), Z7 = cmul(Z4, Z3), Z8 = cmul(Z4, Z4);
    pqw4<24>(m0v, Z4, cmul(Z4, p4), Z5, cmul(Z5, p4));
    pqw4<28>(m0v, Z6, cmul(Z6, p4), Z7, cmul(Z7, p4));
    pqw4<32>(m0v, Z8, cmul(Z8, Z1), cmul(Z8, Z2), cmul(Z8, Z3));
    pqw4<36>(m0v, cmul(Z8, Z4), cmul(Z8, Z5), cmul(Z8, Z6), cmul(Z8, Z7));
    pqw1<40>(m0v, cmul(Z8, Z8));
    STAMP(20);
    kt0 = gld<v4u>(&g_ktab, (u32)t8 * 32u);
    kt1 = gld<v2u>(&g_ktab, (u32)t8 * 32u + 16u);
    LD_HANDOFF_A();
    PIN();
    asm volatile("s_waitcnt vmcnt(10) ; PUBWAIT" ::: "memory");
    BAR();
    if (tid == 0) st4(rs, WS_WRDY + m * 16, tag);
    STAMP(21);
    {
      const f32x4 mx4 = *(LAS f32x4 *)(S + LD_WMAX + (w & 4) * 4);
      const float mx = fmaxf(fmaxf(mx4[0], mx4[1]), fmaxf(mx4[2], mx4[3]));
      ebW = (int)((__builtin_bit_cast(u32, mx) >> 23) & 0xFF);
      ebW = ebW < 14 ? 14 : (ebW > 253 ? 253 : ebW);
    }
    const f2 z16 = cmul(z8, z8), z24 = cmul(z16, z8), z32 = cmul(z16, z16);
    f2 q0 = Wn * pow2b(267 - ebW), q1 = cmul(q0, z8), q2 = cmul(q0, z16), q3 = cmul(q0, z24);
    pqw4<41>(m0v, q0, q1, q2, q3);
    q0 = cmul(q0, z32); q1 = cmul(q1, z32); q2 = cmul(q2, z32); q3 = cmul(q3, z32);
    pqw4<45>(m0v, q0, q1, q2, q3);
    q0 = cmul(q0, z32); q1 = cmul(q1, z32); q2 = cmul(q2, z32); q3 = cmul(q3, z32);
    pqw4<49>(m0v, q0, q1, q2, q3);
    q0 = cmul(q0, z32); q1 = cmul(q1, z32); q2 = cmul(q2, z32); q3 = cmul(q3, z32);
    pqw4<53>(m0v, q0, q1, q2, q3);
  }
  STAMP(4);
  BAR();
  {
    const int tile = w & 3;
    if (tile < 3) {
      LAS unsigned char *pa = S + LD_PQ + cch * PQ_CH + (16 * tile + (lane & 15)) * PQ_ROW + (lane >> 4) * 16;
      LAS unsigned char *pb = S + LD_PQ + cch * PQ_CH + (41 + (lane & 15)) * PQ_ROW + (lane >> 4) * 16;
      f32x4 acc0 = {0.f, 0.f, 0.f, 0.f}, acc1 = acc0;
      v4u fa[16], fb[16];
#pragma unroll
      for (int ks = 0; ks < 16; ks++) { fa[ks] = *(LAS v4u *)(pa + ks * 64); fb[ks] = *(LAS v4u *)(pb + ks * 64); }
      PIN();
#pragma unroll
      for (int ks = 0; ks < 16; ks += 2) { acc0 = mfma16h(fa[ks], fb[ks], acc0); acc1 = mfma16h(fa[ks + 1], fb[ks + 1], acc1); }
      acc0 = (acc0 + acc1) * (1.0f / 256.0f);
      LAS u16 *kd = (LAS u16 *)(S + LD_KD + cch * KD_CH) + (16 * tile + 4 * (lane >> 4)) * 16 + (lane & 15);
      const u32 k01 = pkh(acc0[0], acc0[1]), k23 = pkh(acc0[2], acc0[3]);
      kd[0] = (u16)k01; kd[16] = (u16)(k01 >> 16); kd[32] = (u16)k23; kd[48] = (u16)(k23 >> 16);
    } else if (lane < 16) {
      ((LAS u16 *)(S + LD_KD + cch * KD_CH))[768 + lane] = 0;
    }
  }
#pragma unroll 1
  for (unsigned sp = 0; sp < SPIN_CAP; sp++) {
    const u32 d = ((xg[0].y ^ tag) | (xg[0].w ^ tag) | (xg[1].y ^ tag) | (xg[1].w ^ tag)) |
                  ((sg[0].y ^ tag) | (sg[0].w ^ tag) | (sg[1].y ^ tag) | (sg[1].w ^ tag)) |
                  ((mg[0].y ^ tag) | (mg[0].w ^ tag) | (mg[1].y ^ tag) | (mg[1].w ^ tag)) |
                  ((mg[2].y ^ tag) | (mg[2].w ^ tag) | (mg[3].y ^ tag) | (mg[3].w ^ tag));
    if (__all(d == 0)) break;
    __builtin_amdgcn_s_sleep(1);
    LD_HANDOFF_A();
  }
#undef LD_HANDOFF_A
  __builtin_amdgcn_fence(__ATOMIC_ACQUIRE, "wavefront");
  *(LAS f32x4 *)(S + LD_ST1 + tid * 16) = f32x4{bflo(sg[0].x), __builtin_amdgcn_rcpf(bfhi(sg[0].x)), bflo(sg[0].z), __builtin_amdgcn_rcpf(bfhi(sg[0].z))};
  *(LAS f32x4 *)(S + LD_ST1 + 8192 + tid * 16) = f32x4{bflo(sg[1].x), __builtin_amdgcn_rcpf(bfhi(sg[1].x)), bflo(sg[1].z), __builtin_amdgcn_rcpf(bfhi(sg[1].z))};
  STAMP(5);
  BAR();
  const int fo = WS_WRDY + t8 * 16;
  u32 wr = __builtin_amdgcn_raw_buffer_load_b32(rs, fo, 0, SC1);
  PIN();

  {
    LAS unsigned char *kd = S + LD_KD + cch * KD_CH;
    u32 v[11];
    const u32 ix[6] = {kt0.x, kt0.y, kt0.z, kt0.w, kt1.x, kt1.y};
#pragma unroll
    for (int i = 0; i < 11; i++) v[i] = *(LAS u16 *)(kd + ((i & 1) ? (ix[i >> 1] >> 16) : (ix[i >> 1] & 0xFFFFu)));
    LAS unsigned char *kc = S + LD_KC + cch * KC_CH + (16 + 8 * t8) * 2;
    u32 pr[10];
#pragma unroll
    for (int i = 0; i < 10; i++) pr[i] = v[i] | (v[i + 1] << 16);
#pragma unroll
    for (int e = 0; e < 4; e++) *(LAS v4u *)(kc + e * KC_CP) = v4u{pr[3 - e], pr[5 - e], pr[7 - e], pr[9 - e]};
    if (t8 < 8) *(LAS v4u *)(S + LD_KC + cch * KC_CH + (t8 >> 1) * KC_CP + (t8 & 1) * 16) = v4u{0, 0, 0, 0};
  }
  float A1, A2, c2;
  {
    const float sah = asf(hch ? mg[0].z : mg[0].x), c1h = rbf(1.0f + asf(hch ? mg[1].z : mg[1].x));
    int eh = (int)((__builtin_bit_cast(u32, 21.5f * fabsf(c1h) + fabsf(sah)) >> 23) & 0xFF);
    eh = eh < 14 ? 14 : (eh > 253 ? 253 : eh);
    const float sH = pow2b(267 - eh);
    const f2 c1s = {c1h * sH, c1h * sH}, sas = {sah * sH, sah * sH};
#pragma unroll
    for (int i = 0; i < 2; i++) {
      const u32 x01 = xg[i].x, x23 = xg[i].z;
      const f32x4 sa_ = *(LAS f32x4 *)(S + LD_ST1 + (l4 + 1024 * i) * 8), sb_ = *(LAS f32x4 *)(S + LD_ST1 + (l4 + 1024 * i) * 8 + 16);
      const f2 h01 = __builtin_elementwise_fma((f2{bflo(x01), bfhi(x01)} - f2{sa_[0], sa_[2]}) * f2{sa_[1], sa_[3]}, c1s, sas);
      const f2 h23 = __builtin_elementwise_fma((f2{bflo(x23), bfhi(x23)} - f2{sb_[0], sb_[2]}) * f2{sb_[1], sb_[3]}, c1s, sas);
      *(LAS v2u *)(S + LD_U + hch * U_CH + (256 + l4 + 1024 * i) * 2) = v2u{pkh(h01.x, h01.y), pkh(h23.x, h23.y)};
      *(LAS v2u *)(S + LD_XS + hch * 4096 + (l4 + 1024 * i) * 2) = v2u{x01, x23};
    }
    if (t8 < 32) *(LAS v4u *)(S + LD_U + cch * U_CH + 16 * t8) = v4u{0, 0, 0, 0};
    const float sac = asf(cch ? mg[0].z : mg[0].x), c1c = rbf(1.0f + asf(cch ? mg[1].z : mg[1].x));
    int ec = (int)((__builtin_bit_cast(u32, 21.5f * fabsf(c1c) + fabsf(sac)) >> 23) & 0xFF);
    ec = ec < 14 ? 14 : (ec > 253 ? 253 : ec);
    const float ga = asf(cch ? mg[2].z : mg[2].x);
    const float iH = pow2b(ec - 13);
    A1 = ga * iH * pow2b(ebW - 5);
    A2 = ga * bf2f(pDsk) * iH;
    c2 = rbf(1.0f + asf(cch ? mg[3].z : mg[3].x));
  }
#pragma unroll 1
  for (unsigned sp = 0; sp < SPIN_CAP; sp++) {
    if (__all(wr == tag)) break;
    __builtin_amdgcn_s_sleep(1);
    wr = __builtin_amdgcn_raw_buffer_load_b32(rs, fo, 0, SC1);
  }
  __builtin_amdgcn_fence(__ATOMIC_ACQUIRE, "wavefront");
  STAMP(6);
  BAR();
  if (b == 0 && tid == 64) st4(rs, WS_EPOCH, tag);
  if (w < 7) st16(rs, WS_WDF + ((7 * (m & 1) + w) * 112 + (m >> 1)) * 1024 + lane16, wdfrag);
  PIN();

  const int fc = 7 * (g & 1) + r % 7, tt = 4 * (g >> 1) + r / 7;
  const int phi = 8 * fc + w;
  const int dtile = r % 7, t2 = 4 * g + r / 7;
  v4u w1[28];
  const int w1o = WS_W1F + phi * 28 * 1024;
#define LDW1(s) do { w1[s] = ld16s(rs, lane16, w1o + (s) * 1024); PIN(); } while (0)
  v2u csg_;
  const int cso = WS_CSB + (phi * 64 + lane) * 8;

  {
    const int c = cch, pq = w & 3;
    const int a15 = lane & 15, q = lane >> 4, e = (lane >> 2) & 3, pp = lane & 3;
    LAS unsigned char *ka = S + LD_KC + c * KC_CH + e * KC_CP + (16 + 16 * (q >> 1) - 8 * (q & 1) + 4 * pp) * 2 + pq * 128 - 8;
    LAS unsigned char *ub = S + LD_U + c * U_CH + 512 + 32 * a15 - 32 * (q >> 1) + 16 * (q & 1) - pq * 128 - 64;
    const int l16 = 16 * a15 + 4 * q;
    const int xoff = ((q & 1) ? WS_X1S : WS_X1T) + ((a15 >> 2) * D + dch) * 128 + (16 * (a15 & 3) + 8 * (q >> 1)) * 2;
#define RD_A(i) tr8(ka + (i) * 512 + pi * 64 + 8, ka + (i) * 512 + pi * 64)
#define RD_B(i) (*(LAS v4u *)(ub + (i) * 512 + 64 - pi * 64))
#pragma unroll
    for (int hf = 0; hf < 2; hf++) {
      LAS unsigned char *ex = S + LD_EX + hf * 32768 + c * 16384 + lane16;
      f32x4 acc[4];
#pragma unroll
      for (int i = 0; i < 4; i++) acc[i] = f32x4{0.f, 0.f, 0.f, 0.f};
#pragma unroll
      for (int pi = 0; pi < 2; pi++) {
        v4u A[8], B[8];
        if (hf == 0) {
          A[0] = RD_A(0); B[0] = RD_B(0); B[1] = RD_B(1); B[2] = RD_B(2); B[3] = RD_B(3);
          A[1] = RD_A(1); A[2] = RD_A(2); A[3] = RD_A(3);
        } else {
          A[0] = RD_A(0); B[4] = RD_B(4); B[5] = RD_B(5); B[6] = RD_B(6); B[7] = RD_B(7);
          A[1] = RD_A(1); B[3] = RD_B(3); A[2] = RD_A(2); B[2] = RD_B(2); A[3] = RD_A(3); B[1] = RD_B(1);
          A[4] = RD_A(4); B[0] = RD_B(0); A[5] = RD_A(5); A[6] = RD_A(6); A[7] = RD_A(7);
        }
        PIN();
#pragma unroll
        for (int qq = 0; qq < 8; qq++) {
#pragma unroll
          for (int tl = 0; tl < 4; tl++)
            if (qq <= 4 * hf + tl) acc[tl] = mfma16h(A[qq], B[4 * hf + tl - qq], acc[tl]);
          PIN();
          if (hf == 0 && qq < 2) LDW1(2 * pi + qq);
          if (hf == 1 && qq < 7) LDW1(4 + 7 * pi + qq);
        }
      }
      STAMP(17 + 2 * hf);
#pragma unroll
      for (int tl = 0; tl < 4; tl++) *(LAS f32x4 *)(ex + (pq * 4 + tl) * 1024) = acc[tl];
      if (hf == 1) asm volatile("s_waitcnt vmcnt(14) ; PUBWAIT" ::: "memory");
      BAR();
      if (hf == 1) {
        if (tid < 8) __hip_atomic_fetch_add((gu32 *)(p.ws + WS_CNT) + (0 * 128 + tid * 16 + (m & 15)) * 64, 1u, RLX_AGENT);
        csg_ = ld8(rs, cso);
      }
      const int t = 4 * hf + pq;
      const int l0 = 256 * t + l16;
      const f32x4 y0 = *(LAS f32x4 *)(ex + (0 * 4 + pq) * 1024), y1 = *(LAS f32x4 *)(ex + (1 * 4 + pq) * 1024);
      const f32x4 y2 = *(LAS f32x4 *)(ex + (2 * 4 + pq) * 1024), y3 = *(LAS f32x4 *)(ex + (3 * 4 + pq) * 1024);
      const v2u hh = *(LAS v2u *)(S + LD_U + c * U_CH + (256 + l0) * 2);
      const v2u xx = *(LAS v2u *)(S + LD_XS + c * 4096 + l0 * 2);
      const f32x4 y = (y0 + y1) + (y2 + y3);
      const h2 ha = ash2(hh.x), hb = ash2(hh.y);
      const f2 a1 = {A1, A1}, a2 = {A2, A2}, cc = {c2, c2};
      const f2 o01 = __builtin_elementwise_fma(a2, f2{(float)ha.x, (float)ha.y},
                         __builtin_elementwise_fma(a1, f2{y[0], y[1]}, f2{bflo(xx.x), bfhi(xx.x)}));
      const f2 o23 = __builtin_elementwise_fma(a2, f2{(float)hb.x, (float)hb.y},
                         __builtin_elementwise_fma(a1, f2{y[2], y[3]}, f2{bflo(xx.y), bfhi(xx.y)}));
      const u32 X0 = pkbf(o01.x, o01.y), X1 = pkbf(o23.x, o23.y);
      const f2 s01 = f2{bflo(X0), bfhi(X0)} * cc, s23 = f2{bflo(X1), bfhi(X1)} * cc;
      const u32 Y0 = pkbf(s01.x, s01.y), Y1 = pkbf(s23.x, s23.y);
      const auto r0 = __builtin_amdgcn_permlane16_swap(X0, Y0, false, false);
      const auto r1 = __builtin_amdgcn_permlane16_swap(X1, Y1, false, false);
      st16(rs, xoff + 4 * t * D * 128, v4u{r0[0], r1[0], r0[1], r1[1]});
      PIN();
      STAMP(18 + 4 * hf);
    }
#pragma unroll
    for (int i = 18; i < 24; i++) LDW1(i);
    asm volatile("s_waitcnt vmcnt(6) ; PUBWAIT" ::: "memory");
    BAR();
    if (tid < 8) __hip_atomic_fetch_add((gu32 *)(p.ws + WS_CNT) + (1 * 128 + tid * 16 + (m & 15)) * 64, 1u, RLX_AGENT);
#undef RD_A
#undef RD_B
  }
  STAMP(7);

  if (w == 0) {
    poll_counters(p, 0 * 128 + g * 16, 16, 14u, lane);
  } else {
#pragma unroll
    for (int i = 24; i < 28; i++) LDW1(i);
  }
  BAR();
  STAMP(8);
  v4u xs[7];
  const int xso = tid * 16;
  const int xlo = (tid >> 3) * 128 + (((tid & 7) ^ (((tid >> 4) & 1) << 2)) * 16);
#pragma unroll
  for (int i = 0; i < 7; i++) xs[i] = ld16s(rs, xso, WS_X1S + tt * XB_SZ + i * 8192);
  PIN();
  if (w == 0) {
#pragma unroll
    for (int i = 24; i < 28; i++) LDW1(i);
  }
  *(LAS u32 *)(S + LD_CSL + (w * 64 + lane) * 4) = finish_granule(rs, cso, tag, csg_);
#define NWA 4
#define NWB 4
  constexpr int NWPF = NWA + NWB;
  v4u wd[28];
  const int wdo = WS_WDF + (2 * dtile * 112 + 14 * w) * 1024;
#define LDWD(i) (wd[i] = ld16s(rs, lane16, wdo + (((i) / 14) * 112 + (i) % 14) * 1024))
#pragma unroll
  for (int i = 0; i < 7; i++) {
    *(LAS v4u *)(S + LD_XB + xlo + i * 8192) = xs[i];
    if (i < NWA) LDWD(i);
    PIN();
  }
  BAR();
  STAMP(9);
  v2u sg2[2];
  u32 pk;
  {
    const int G = lane >> 4, e = (lane >> 2) & 3, pp = lane & 3, h = lane >> 5;
    const int sw = ((e >> 1) & 1) * 64;
    LAS unsigned char *bx0 = S + LD_XB + (8 * h + e) * 128 + ((0 + 32 * (G & 1) + 8 * pp) ^ sw);
    LAS unsigned char *bx1 = S + LD_XB + (8 * h + e) * 128 + ((64 + 32 * (G & 1) + 8 * pp) ^ sw);
    LAS unsigned char *cl = S + LD_CSL + w * 256 + 16 * h;
    const int ho = WS_HF + phi * 1024 + lane16;
    f32x16 acc[2][2];
    f2 ep1[2], ep2[2];
    u32 hp[2][4];
#define EPI_PART(hf_, j, gq) do { \
      const f32x4 csg = *(LAS f32x4 *)(cl + 32 * (gq)), csu = *(LAS f32x4 *)(cl + 32 * (gq) + 64); \
      const f32x4 bg = *(LAS f32x4 *)(cl + 32 * (gq) + 128), bu = *(LAS f32x4 *)(cl + 32 * (gq) + 192); \
      _Pragma("unroll") for (int i = 0; i < 4; i += 2) { \
        const f2 gg = __builtin_elementwise_fma(ep1[j], f2{acc[hf_][j][4 * (gq) + i], acc[hf_][j][4 * (gq) + i + 1]}, \
                          __builtin_elementwise_fma(ep2[j], f2{csg[i], csg[i + 1]}, f2{bg[i], bg[i + 1]})); \
        const f2 uu = __builtin_elementwise_fma(ep1[j], f2{acc[hf_][j][8 + 4 * (gq) + i], acc[hf_][j][8 + 4 * (gq) + i + 1]}, \
                          __builtin_elementwise_fma(ep2[j], f2{csu[i], csu[i + 1]}, f2{bu[i], bu[i + 1]})); \
        const f2 hv = gg * uu; \
        hp[j][2 * (gq) + i / 2] = pkbf(hv.x, hv.y); \
      } } while (0)
#define EPI_STORE(hf_, j) st16(rs, ho + (2 * (16 * (hf_) + tt) + (j)) * 112 * 1024, v4u{hp[j][0], hp[j][1], hp[j][2], hp[j][3]})
#define EPI_STATS(hf_) do { \
      const int sto_ = WS_STAT2 + (64 * (16 * (hf_) + tt) + (lane & 31)) * 8; \
      const u32 sv0 = finish_granule(rs, sto_, tag, sg2[0]), sv1 = finish_granule(rs, sto_ + 256, tag, sg2[1]); \
      const float r0_ = __builtin_amdgcn_rcpf(bfhi(sv0)), r1_ = __builtin_amdgcn_rcpf(bfhi(sv1)); \
      ep1[0] = f2{r0_, r0_}; ep1[1] = f2{r1_, r1_}; \
      ep2[0] = f2{-r0_ * bflo(sv0), -r0_ * bflo(sv0)}; ep2[1] = f2{-r1_ * bflo(sv1), -r1_ * bflo(sv1)}; } while (0)
    constexpr int NREM = 28 - NWPF;
#define WDSTEP(s) (((s) * NREM / 27 != ((s) + 1) * NREM / 27 && (s) < 27) ? NWPF + (s) * NREM / 27 : -1)
    constexpr int PUBSTEP = 10, PUBCNT = (PUBSTEP + 1) * NREM / 27;
#pragma unroll
    for (int hf = 0; hf < 2; hf++) {
#pragma unroll
      for (int j = 0; j < 2; j++)
#pragma unroll
        for (int i = 0; i < 16; i++) acc[hf][j][i] = 0.f;
      v4u fb0[3], fb1[3];
      LAS unsigned char *bh0 = bx0 + hf * XB_SZ, *bh1 = bx1 + hf * XB_SZ;
      asm volatile("" : "+v"(bh0), "+v"(bh1));
#define RD_X(s) do { const int o_ = (s) * 2048; fb0[(s) % 3] = tr8(bh0 + o_, bh0 + o_ + 512); fb1[(s) % 3] = tr8(bh1 + o_, bh1 + o_ + 512); } while (0)
      RD_X(0); RD_X(1);
#pragma unroll
      for (int s = 0; s < 28; s++) {
        if (s + 2 < 28) RD_X(s + 2);
        if (hf == 0) {
          if ((s & 1) == 0 && s / 2 < NWB) LDWD(NWA + s / 2);
          if (s == 3 && w == 0) pk = peek_counters(p, 1 * 128 + g * 16, 16, lane);
          if (s == 9) {
            if (w == 0) finish_counters(p, 1 * 128 + g * 16, 16, 14u, lane, pk);
            BAR();
          }
          if (s >= 9 && s < 16) xs[s - 9] = ld16s(rs, xso, WS_X1S + (16 + tt) * XB_SZ + (s - 9) * 8192);
          if (s >= 21) *(LAS v4u *)(S + LD_XB + XB_SZ + xlo + (s - 21) * 8192) = xs[s - 21];
        } else {
          if (WDSTEP(s) >= 0) LDWD(WDSTEP(s) < 0 ? 0 : WDSTEP(s));
          if (s == PUBSTEP) {
            asm volatile("s_waitcnt vmcnt(%0) ; PUBWAIT" ::"n"(PUBCNT) : "memory");
            BAR();
            if (tid == 0) __hip_atomic_fetch_add((gu32 *)(p.ws + WS_CNT) + (256 + tt) * 64, 1u, RLX_AGENT);
          }
        }
        if (s == 20) {
          const int sto = WS_STAT2 + (64 * (16 * hf + tt) + (lane & 31)) * 8;
          sg2[0] = ld8(rs, sto); sg2[1] = ld8(rs, sto + 256);
        }
        PIN();
        acc[hf][0] = mfma32b(w1[s], fb0[s % 3], acc[hf][0]);
        acc[hf][1] = mfma32b(w1[s], fb1[s % 3], acc[hf][1]);
        PIN();
        if (s == 3) STAMP(23 + 3 * hf);
        if (s == 27) STAMP(24 + 3 * hf);
      }
#undef RD_X
      if (hf == 0) {
        EPI_STATS(0);
        EPI_PART(0, 0, 0); EPI_PART(0, 0, 1); EPI_PART(0, 1, 0); EPI_PART(0, 1, 1);
        PIN();
        EPI_STORE(0, 0); EPI_STORE(0, 1);
        PIN();
        BAR();
        STAMP(10);
      }
    }
    EPI_STATS(1);
    STAMP(28);
    EPI_PART(1, 0, 0); EPI_PART(1, 0, 1); EPI_PART(1, 1, 0); EPI_PART(1, 1, 1);
    if (w == 0) pk = peek_counters(p, 256 + (t2 >> 1), 1, lane);
    PIN();
    EPI_STORE(1, 0); EPI_STORE(1, 1);
    PIN();
    STAMP(11);
#undef EPI_PART
#undef EPI_STORE
#undef EPI_STATS
  }

  if (w == 0) finish_counters(p, 256 + (t2 >> 1), 1, 14u, lane, pk);
  BAR();
  STAMP(12);
  v4u gbg[2], hb[14], res[2];
  const int gbo = WS_MODG + (5 * D + 64 * dtile + 4 * (2 * w + (lane >> 5))) * 8;
  const int reso = WS_X1T + ((t2 >> 1) * D + 64 * dtile + ((tid & 255) >> 2)) * 128 + (t2 & 1) * 64 + (tid & 3) * 16;
  const int hbo = WS_HF + (t2 * 112 + 14 * w) * 1024;
#pragma unroll
  for (int k = 0; k < 5; k++) hb[k] = ld16s(rs, lane16, hbo + k * 1024);
  PIN();
  asm volatile("s_waitcnt vmcnt(5) ; PUBWAIT" ::: "memory");
  BAR();
  if (tid == 0) __hip_atomic_fetch_add((gu32 *)(p.ws + WS_CNT) + (256 + 16 + tt) * 64, 1u, RLX_AGENT);
#pragma unroll
  for (int k = 5; k < 14; k++) hb[k] = ld16s(rs, lane16, hbo + k * 1024);
  res[0] = ld16s(rs, reso, 0); res[1] = ld16s(rs, reso, 16 * D * 128);
  gbg[0] = ld16(rs, gbo); gbg[1] = ld16(rs, gbo + 16);
  PIN();
#pragma unroll
  for (int hf = 0; hf < 2; hf++) {
    f32x16 acc[2];
#pragma unroll
    for (int j = 0; j < 2; j++)
#pragma unroll
      for (int i = 0; i < 16; i++) acc[j][i] = 0.f;
#pragma unroll
    for (int k = 0; k < 14; k++) {
      acc[0] = mfma32b(wd[k], hb[k], acc[0]);
      acc[1] = mfma32b(wd[14 + k], hb[k], acc[1]);
      if (hf == 0 && k == 9 && w == 0) { PIN(); pk = peek_counters(p, 256 + 16 + (t2 >> 1), 1, lane); PIN(); }
    }
    PIN();
    STAMP(29 + hf);
#define LDHL(k) do { if (hf == 0) { hb[k] = ld16s(rs, lane16, hbo + (32 * 112 + (k)) * 1024); PIN(); } } while (0)
    if (hf == 0) {
      if (w == 0) finish_counters(p, 256 + 16 + (t2 >> 1), 1, 14u, lane, pk);
      BAR();
    }
    {
      const int col = lane & 31, h = lane >> 5;
      LAS unsigned char *rd = S + LD_RED + w * RED_W + col * RED_ROW + h * 16;
#pragma unroll
      for (int i = 0; i < 8; i++) {
        const int dl = i >> 2, gq = i & 3;
        *(LAS f32x4 *)(rd + dl * 128 + gq * 32) = f32x4{acc[dl][4 * gq], acc[dl][4 * gq + 1], acc[dl][4 * gq + 2], acc[dl][4 * gq + 3]};
        PIN();
        LDHL(i);
      }
      if (tid < 256) *(LAS v4u *)(S + LD_RES + (tid >> 2) * 64 + (tid & 3) * 16) = res[hf];
    }
    if (hf == 0) {
#pragma unroll 1
      for (unsigned sp = 0; sp < SPIN_CAP; sp++) {
        if (__all(gbg[0].y == tag && gbg[0].w == tag && gbg[1].y == tag && gbg[1].w == tag)) break;
        gbg[0] = ld16(rs, gbo); gbg[1] = ld16(rs, gbo + 16);
      }
    }
    BAR();
    {
      const int tok = lane & 31, dq = 2 * w + (lane >> 5);
      f32x4 part[8];
#pragma unroll
      for (int ww = 0; ww < 8; ww++) part[ww] = *(LAS f32x4 *)(S + LD_RED + ww * RED_W + tok * RED_ROW + dq * 16);
      const v2u rr = trread(S + LD_RES + (4 * dq + ((lane >> 2) & 3)) * 64 + (16 * ((lane >> 4) & 1) + 4 * (lane & 3)) * 2);
      PIN();
      LDHL(8); LDHL(9);
      const f32x4 s0 = (part[0] + part[1]) + (part[2] + part[3]);
      PIN();
      LDHL(10); LDHL(11);
      const f32x4 sum = s0 + ((part[4] + part[5]) + (part[6] + part[7]));
      PIN();
      LDHL(12); LDHL(13);
      const u32 b01 = pkbf(sum[0], sum[1]), b23 = pkbf(sum[2], sum[3]);
      const u32 t01 = pkbf(asf(gbg[0].x) * bflo(b01), asf(gbg[0].z) * bfhi(b01));
      const u32 t23 = pkbf(asf(gbg[1].x) * bflo(b23), asf(gbg[1].z) * bfhi(b23));
      const float o0 = bflo(rr.x) + bflo(t01), o1 = bfhi(rr.x) + bfhi(t01);
      const float o2 = bflo(rr.y) + bflo(t23), o3 = bfhi(rr.y) + bfhi(t23);
      *(GAS v2u *)((GAS char *)p.out + (u32)((32 * (32 * hf + t2) + tok) * D + 64 * dtile + 4 * dq) * 2u) = v2u{pkbf(o0, o1), pkbf(o2, o3)};
    }
#undef LDHL
    STAMP(13 + 2 * hf);
    if (hf == 0) BAR();
  }
#undef LDW1
}

__device__ __forceinline__ void side_role(const Params &p, LAS unsigned char *S, const rsrc_t rs, const int tid,
                                          const int lane, const int w, const int b, const u32 epoch, unsigned long long &probe_t) {
  const int g = b & 7, r = b >> 3, sidx = 4 * g + r - 28;
  const int sc = sidx < 28 ? sidx : 27;
  const int lane16 = lane * 16;
  const int tau = 16 * g + (r >> 1);
  const u32 xo = (u32)(16 * tau + 2 * w) * (2u * D) + (u32)(lane < 56 ? lane : 55) * 16u;
  const v4u x0 = gld<v4u>(p.x, xo), x1 = gld<v4u>(p.x, xo + 2u * D);
  const u32 tag = __builtin_amdgcn_readfirstlane(epoch) + 1u;
  if (tid == 0) st4(rs, WS_WRDY + (224 + sidx) * 16, tag);
  if ((r & 1) == 0) {
    ln1_rows(S, x0, x1, w, lane);
    STAMP(1);
    BAR();
    publish_xt(S, rs, lane, w, tau, tag);
  }
  STAMP(3);
  {
    const int dd = tid < D ? tid : 0;
    const int o1 = WS_WRDY + (tid < 224 ? tid : 0) * 16, o2 = WS_MODG + (4 * D + dd) * 8, o3 = WS_MODG + (3 * D + dd) * 8;
    const v2u g2 = ld8(rs, o2), g3 = ld8(rs, o3);
    const u32 vcb = finish_granule(rs, o2, tag, g2), vsb = finish_granule(rs, o3, tag, g3);
    if (tid < D) {
      ((LAS u16 *)(S + LD_CV))[tid] = (u16)(pkbf(1.0f + asf(vcb), 0.f) & 0xFFFFu);
      ((LAS u16 *)(S + LD_CV))[D + tid] = (u16)(vsb >> 16);
    }
    if (w < 4) {
#pragma unroll 1
      for (unsigned sp = 0; sp < SPIN_CAP; sp++) {
        const u32 wr = __builtin_amdgcn_raw_buffer_load_b32(rs, o1, 0, SC1);
        if (__all(wr == tag)) break;
        __builtin_amdgcn_s_sleep(4);
      }
      __builtin_amdgcn_fence(__ATOMIC_ACQUIRE, "wavefront");
    }
  }
  BAR();
  STAMP(5);
  {
    const int ph = 4 * sc + (w >> 1), s0 = 14 * (w & 1);
    v4u wf[14];
#pragma unroll
    for (int k = 0; k < 14; k++) wf[k] = ld16s(rs, lane16, WS_W1F + (ph * 28 + s0 + k) * 1024);
    PIN();
    f32x16 acc;
#pragma unroll
    for (int i = 0; i < 16; i++) acc[i] = 0.f;
    LAS unsigned char *cvp = S + LD_CV + ((lane & 31) == 1 ? D * 2 : 0) + (lane >> 5) * 16;
#pragma unroll
    for (int k = 0; k < 14; k++) acc = mfma32b(wf[k], *(LAS v4u *)(cvp + (s0 + k) * 32), acc);
    LAS float *pr = (LAS float *)(S) + w * 64;
    if ((lane & 31) < 2) {
#pragma unroll
      for (int i = 0; i < 16; i++) pr[(lane & 31) * 32 + (i & 3) + 8 * (i >> 2) + 4 * (lane >> 5)] = acc[i];
    }
  }
  BAR();
  if (sidx < 28 && tid < 256) {
    const int pl = tid >> 6, e = tid & 63;
    const float v = ((LAS float *)S)[(2 * pl) * 64 + e] + ((LAS float *)S)[(2 * pl + 1) * 64 + e];
    st8(rs, WS_CSB + ((4 * sidx + pl) * 64 + e) * 8, v2u{__builtin_bit_cast(u32, v), tag});
  }
  STAMP(6);
#pragma unroll 1
  for (int hf = 0; hf < 2; hf++) {
    if (w == 0) poll_counters(p, hf * 128 + g * 16, 16, 14u, lane);
    BAR();
    const int tau2 = 32 * hf + sidx;
    const int tp = lane & 15, rgp = (lane >> 4) + 4 * w;
    u32 xv[14];
#pragma unroll
    for (int i = 0; i < 14; i++)
      xv[i] = __builtin_amdgcn_raw_buffer_load_b32(rs, rgp * 128 + tp * 4, WS_X1T + ((tau2 >> 1) * D + 32 * i) * 128 + (tau2 & 1) * 64, SC1);
    PIN();
    f2 sm = {0.f, 0.f}, sq = {0.f, 0.f};
#pragma unroll
    for (int i = 0; i < 14; i++) {
      const f2 a = {bflo(xv[i]), bfhi(xv[i])};
      sm += a; sq = __builtin_elementwise_fma(a, a, sq);
    }
    LAS f32x4 *pr = (LAS f32x4 *)(S + 4096);
    pr[rgp * 16 + tp] = f32x4{sm.x, sm.y, sq.x, sq.y};
    BAR();
    if (tid < 128) {
      const int tok = tid >> 2, part = tid & 3;
      LAS float *pf = (LAS float *)pr + (part * 8 * 16 + (tok >> 1)) * 4 + (tok & 1);
      float s = 0.f, q = 0.f;
#pragma unroll
      for (int i = 0; i < 8; i++) { s += pf[i * 64]; q += pf[i * 64 + 2]; }
      s += dpp0<0xB1, 0xF>(s); q += dpp0<0xB1, 0xF>(q);
      s += dpp0<0x4E, 0xF>(s); q += dpp0<0x4E, 0xF>(q);
      const float mean = s * (1.0f / D);
      const float var = rbf(fmaxf(q * (1.0f / D) - mean * mean, 0.f));
      const float sd = rbf(__builtin_amdgcn_sqrtf(rbf(var + 1e-5f)));
      if (part == 0) st8(rs, WS_STAT2 + (32 * tau2 + tok) * 8, v2u{pkbf(mean, sd), tag});
    }
    STAMP(8 + hf);
  }
}

__global__ void __launch_bounds__(NTHR, 2) k_main(Params p) {
  extern __shared__ __attribute__((aligned(16))) unsigned char smem[];
  LAS unsigned char *S = (LAS unsigned char *)smem;
  const int tid = threadIdx.x, lane = tid & 63, b = blockIdx.x;
  const int w = __builtin_amdgcn_readfirstlane(tid >> 6);
  const rsrc_t rs = __builtin_amdgcn_make_buffer_rsrc((void *)p.ws, 0, WS_END, 0x00020000);
  u32 pf;
  {
    unsigned long long pc;
    asm volatile("s_getpc_b64 %0" : "=s"(pc));
    pf = gld<u32>((const void *)(pc & ~127ull), (u32)(tid < PF_LINES ? tid : 0) * 128u);
  }
  const u32 epoch = __builtin_amdgcn_raw_buffer_load_b32(rs, WS_EPOCH, 0, SC1);
  unsigned long long probe_t = 0;
  const unsigned long long probe_0 = __builtin_amdgcn_s_memrealtime();
  STAMP(0);
  if ((b >> 3) < 28) main_role(p, S, rs, tid, lane, w, b, epoch, probe_t);
  else side_role(p, S, rs, tid, lane, w, b, epoch, probe_t);
  STAMP(16);
  if (b == MK_PWG && w == 0) {
    const unsigned long long t1 = __builtin_amdgcn_s_memrealtime(), d = (probe_t - probe_0) * 8;
    while (__builtin_amdgcn_s_memrealtime() - t1 < d) __builtin_amdgcn_s_sleep(1);
  }
  asm volatile("" ::"v"(pf));
}

}

extern "C" void kernel_launch(void *const *d_in, const int *in_sizes, int n_in, void *d_out, int out_size, void *d_ws,
                              size_t ws_size, hipStream_t stream) {
  using namespace mk;
  Params p;
  p.x = (const u16 *)d_in[0];
  p.Are = (const u16 *)d_in[1]; p.Aim = (const u16 *)d_in[2];
  p.Bre = (const u16 *)d_in[3]; p.Bim = (const u16 *)d_in[4];
  p.Cre = (const u16 *)d_in[5]; p.Cim = (const u16 *)d_in[6];
  p.logdt = (const u16 *)d_in[7]; p.Dsk = (const u16 *)d_in[8];
  p.wg = (const u16 *)d_in[10]; p.wu = (const u16 *)d_in[11]; p.wd = (const u16 *)d_in[12];
  p.cvec = (const u16 *)d_in[13]; p.wmod = (const u16 *)d_in[14];
  p.out = (u16 *)d_out;
  p.ws = (unsigned char *)d_ws;
  (void)hipFuncSetAttribute((const void *)k_main, hipFuncAttributeMaxDynamicSharedMemorySize, LDS_BYTES);
  k_main<<<NWG, NTHR, LDS_BYTES, stream>>>(p);
}
```

```cpp
#define MUL 0
#include <hip/hip_runtime.h>
#include <stdint.h>

namespace mk {

typedef unsigned short u16;
typedef unsigned u32;
typedef u32 v4u __attribute__((ext_vector_type(4)));
typedef u32 v2u __attribute__((ext_vector_type(2)));
typedef float f2 __attribute__((ext_vector_type(2)));
typedef float f32x4 __attribute__((ext_vector_type(4)));
typedef float f32x16 __attribute__((ext_vector_type(16)));
typedef _Float16 h2 __attribute__((ext_vector_type(2)));
typedef _Float16 h8 __attribute__((ext_vector_type(8)));
typedef __bf16 b2 __attribute__((ext_vector_type(2)));
typedef __bf16 b8 __attribute__((ext_vector_type(8)));
typedef short s4 __attribute__((ext_vector_type(4)));
#define LAS __attribute__((address_space(3)))
#define GAS __attribute__((address_space(1)))
typedef GAS u32 gu32;
#define RLX_AGENT __ATOMIC_RELAXED, __HIP_MEMORY_SCOPE_AGENT

constexpr int L = 2048, D = 448, NS = 256, F = 1792;
constexpr int NWG = 256, NTHR = 512;
constexpr int SC1 = 16;
constexpr int PF_LINES = 190;

constexpr int WS_EPOCH = 0;
constexpr int WS_CNT = 4096;
constexpr int NCNT = 288;
constexpr int WS_STAT1 = WS_CNT + NCNT * 256;
constexpr int WS_MODG = WS_STAT1 + 2048 * 8;
constexpr int WS_CSB = WS_MODG + 2688 * 8;
constexpr int WS_STAT2 = WS_CSB + 112 * 64 * 8;
constexpr int WS_WRDY = WS_STAT2 + 2048 * 8;
constexpr int WS_XG = WS_WRDY + 256 * 16;
constexpr int WS_W1F = WS_XG + 128 * 224 * 128;
constexpr int WS_WDF = WS_W1F + 112 * 28 * 1024;
constexpr int WS_X1T = WS_WDF + 14 * 112 * 1024;
constexpr int WS_X1S = WS_X1T + 32 * 448 * 128;
constexpr int WS_HF = WS_X1S + 32 * 448 * 128;
constexpr int WS_DBG = WS_HF + 64 * 112 * 1024;
constexpr int WS_END = WS_DBG + (8 << 20);
static_assert(WS_STAT1 % 128 == 0 && WS_MODG % 128 == 0 && WS_CSB % 128 == 0 && WS_STAT2 % 128 == 0 && WS_XG % 128 == 0 && WS_W1F % 128 == 0, "align");

constexpr int PQ_ROW = 1056, PQ_CH = 57 * PQ_ROW;
constexpr int LD_PQ = 0;
constexpr int LD_KC = 0;
constexpr int KC_CP = 4160, KC_CH = 4 * KC_CP;
constexpr int LD_EX = 33280;
constexpr int LD_XB = 0;
constexpr int XB_SZ = 448 * 128;
constexpr int LD_RED = 0;
constexpr int RED_ROW = 272, RED_W = 32 * RED_ROW;
constexpr int LD_RES = 8 * RED_W;
constexpr int LD_KD = 120384;
constexpr int KD_CH = 784 * 2;
constexpr int LD_U = LD_KD + 2 * KD_CH;
constexpr int U_CH = 2304 * 2;
constexpr int LD_XS = LD_U + 2 * U_CH;
constexpr int LD_ST1 = 140928;
constexpr int LD_XROW = 120384;
constexpr int LD_WSTG = LD_XROW + 16128;
constexpr int WSTG_ROW = 464, WSTG_M = 16 * WSTG_ROW;
constexpr int LD_SMALL = LD_WSTG + 3 * WSTG_M;
constexpr int LD_STAT = LD_SMALL;
constexpr int LD_WMAX = LD_SMALL + 64;
constexpr int LD_CSL = LD_SMALL + 96;
constexpr int LD_STAMP = LD_CSL + 2048;
constexpr int LD_CV = LD_STAMP + 256;
constexpr int LD_END = LD_CV + 2 * 448 * 2;
constexpr int LDS_BYTES = 160 * 1024;
static_assert(LD_XS + 8192 <= LD_ST1 && LD_ST1 + 16384 <= LD_SMALL && LD_XS + 8192 <= LD_SMALL && LD_END <= LDS_BYTES && LD_CSL % 16 == 0 && LD_CV % 16 == 0, "lds");
static_assert(LD_RES + 4096 <= LD_KD, "lds red");

struct Params {
  const u16 *x, *Are, *Aim, *Bre, *Bim, *Cre, *Cim, *logdt, *Dsk, *wg, *wu, *wd, *cvec, *wmod;
  u16 *out;
  unsigned char *ws;
};

struct KTab { u16 v[256][16]; };
constexpr int bf16_round_int(int j) {
  if (j < 256) return j;
  int sh = 0; int t = j; while (t >= 256) { t >>= 1; sh++; }
  int q = j >> sh, rem = j & ((1 << sh) - 1), half = 1 << (sh - 1);
  if (rem > half || (rem == half && (q & 1))) q++;
  return q << sh;
}
constexpr int kd_index(int j) {
  if (j < 0) return 768;
  int l = bf16_round_int(j);
  if (l >= 2048) return 40 * 16;
  int k = l < 256 ? 0 : (l < 512 ? 1 : (l < 1024 ? 2 : 3));
  int base = k ? (128 << k) : 0;
  int rem = l - base;
  int a = rem >> 7, b = (rem >> 3) & 15, c = (rem & 7) >> k;
  int row = (k ? 8 + 8 * k : 0) + a * (8 >> k) + c;
  return row * 16 + b;
}
constexpr KTab make_ktab() {
  KTab t{};
  for (int i = 0; i < 256; i++)
    for (int j = 0; j < 16; j++) t.v[i][j] = (u16)(2 * (j < 11 ? kd_index(8 * i - 3 + j) : 768));
  return t;
}
__device__ const KTab g_ktab = make_ktab();

__device__ __forceinline__ float asf(u32 v) { return __builtin_bit_cast(float, v); }
__device__ __forceinline__ h2 ash2(u32 v) { return __builtin_bit_cast(h2, v); }
__device__ __forceinline__ float bf2f(u32 v) { return __builtin_bit_cast(float, v << 16); }
__device__ __forceinline__ float bflo(u32 v) { return __builtin_bit_cast(float, v << 16); }
__device__ __forceinline__ float bfhi(u32 v) { return __builtin_bit_cast(float, v & 0xFFFF0000u); }
__device__ __forceinline__ u32 pkbf(float a, float b) {
  return __builtin_bit_cast(u32, __builtin_convertvector(f2{a, b}, b2));
}
__device__ __forceinline__ u32 pkh(float a, float b) {
  return __builtin_bit_cast(u32, __builtin_convertvector(f2{a, b}, h2));
}
__device__ __forceinline__ float rbf(float a) { return bflo(pkbf(a, a)); }
__device__ __forceinline__ f2 cmul(f2 a, f2 b) {
  f2 r;
  asm("v_pk_mul_f32 %0, %1, %2 op_sel_hi:[0,1]\n\t"
      "v_pk_fma_f32 %0, %1, %2, %0 op_sel:[1,1,0] op_sel_hi:[1,0,1] neg_lo:[1,0,0]"
      : "=&v"(r) : "v"(a), "v"(b));
  return r;
}
__device__ __forceinline__ float dot2bf(u32 a, u32 b, float c) {
  return __builtin_amdgcn_fdot2_f32_bf16(__builtin_bit_cast(b2, a), __builtin_bit_cast(b2, b), c, false);
}
template <class T> __device__ __forceinline__ T gld(const void *base, u32 off) { return *(const GAS T *)((const GAS char *)base + off); }
template <int CTRL, int RM> __device__ __forceinline__ float dpp0(float v) {
  return __builtin_bit_cast(float, __builtin_amdgcn_update_dpp(0, __builtin_bit_cast(int, v), CTRL, RM, 0xF, true));
}
__device__ __forceinline__ float wave_sum(float v) {
  v += dpp0<0x111, 0xF>(v);
  v += dpp0<0x112, 0xF>(v);
  v += dpp0<0x114, 0xF>(v);
  v += dpp0<0x118, 0xF>(v);
  v += dpp0<0x142, 0xA>(v);
  v += dpp0<0x143, 0xC>(v);
  return __builtin_bit_cast(float, __builtin_amdgcn_readlane(__builtin_bit_cast(int, v), 63));
}
__device__ __forceinline__ float wave_max_nonneg(float v) {
  v = fmaxf(v, dpp0<0x111, 0xF>(v));
  v = fmaxf(v, dpp0<0x112, 0xF>(v));
  v = fmaxf(v, dpp0<0x114, 0xF>(v));
  v = fmaxf(v, dpp0<0x118, 0xF>(v));
  v = fmaxf(v, dpp0<0x142, 0xA>(v));
  v = fmaxf(v, dpp0<0x143, 0xC>(v));
  return __builtin_bit_cast(float, __builtin_amdgcn_readlane(__builtin_bit_cast(int, v), 63));
}
__device__ __forceinline__ v2u trread(LAS unsigned char *p) {
  return __builtin_bit_cast(v2u, __builtin_amdgcn_ds_read_tr16_b64_v4i16((LAS s4 *)p));
}
__device__ __forceinline__ v4u tr8(LAS unsigned char *p0, LAS unsigned char *p1) {
  v2u a = trread(p0), b = trread(p1);
  return v4u{a.x, a.y, b.x, b.y};
}
__device__ __forceinline__ f32x4 mfma16h(v4u a, v4u b, f32x4 c) {
  return __builtin_amdgcn_mfma_f32_16x16x32_f16(__builtin_bit_cast(h8, a), __builtin_bit_cast(h8, b), c, 0, 0, 0);
}
__device__ __forceinline__ f32x16 mfma32b(v4u a, v4u b, f32x16 c) {
  return __builtin_amdgcn_mfma_f32_32x32x16_bf16(__builtin_bit_cast(b8, a), __builtin_bit_cast(b8, b), c, 0, 0, 0);
}
#define BAR() asm volatile("s_waitcnt lgkmcnt(0)\n\ts_barrier" ::: "memory")
#define DRAIN() asm volatile("s_waitcnt vmcnt(0)" ::: "memory")
#define PIN() do { asm volatile("" ::: "memory"); __builtin_amdgcn_sched_barrier(0); } while (0)
typedef __amdgpu_buffer_rsrc_t rsrc_t;
__device__ __forceinline__ v4u ld16(rsrc_t rs, int off) { return __builtin_amdgcn_raw_buffer_load_b128(rs, off, 0, SC1); }
__device__ __forceinline__ v4u ld16s(rsrc_t rs, int voff, int soff) { return __builtin_amdgcn_raw_buffer_load_b128(rs, voff, soff, SC1); }
__device__ __forceinline__ void st16s(rsrc_t rs, int voff, int soff, v4u v) { __builtin_amdgcn_raw_buffer_store_b128(v, rs, voff + soff, 0, SC1); }
__device__ __forceinline__ v2u ld8(rsrc_t rs, int off) { return __builtin_amdgcn_raw_buffer_load_b64(rs, off, 0, SC1); }
__device__ __forceinline__ void st16(rsrc_t rs, int off, v4u v) { __builtin_amdgcn_raw_buffer_store_b128(v, rs, off, 0, SC1); }
__device__ __forceinline__ void st8(rsrc_t rs, int off, v2u v) { __builtin_amdgcn_raw_buffer_store_b64(v, rs, off, 0, SC1); }
__device__ __forceinline__ void st4(rsrc_t rs, int off, u32 v) { __builtin_amdgcn_raw_buffer_store_b32(v, rs, off, 0, SC1); }

constexpr unsigned SPIN_CAP = 1u << 14;

#define STAMP(i)

__device__ __forceinline__ u32 peek_counters(const Params &p, int idx0, int ns, int lane) {
  return __hip_atomic_load((gu32 *)(p.ws + WS_CNT) + (idx0 + (lane < ns ? lane : 0)) * 64, RLX_AGENT);
}
__device__ __forceinline__ void finish_counters(const Params &p, int idx0, int ns, u32 want, int lane, u32 v) {
#pragma unroll 1
  for (unsigned sp = 0; sp < SPIN_CAP; sp++) {
    if (__all(v >= want)) break;
    __builtin_amdgcn_s_sleep(1);
    v = peek_counters(p, idx0, ns, lane);
  }
  __builtin_amdgcn_fence(__ATOMIC_ACQUIRE, "wavefront");
}
__device__ __forceinline__ void poll_counters(const Params &p, int idx0, int ns, u32 want, int lane) {
  finish_counters(p, idx0, ns, want, lane, peek_counters(p, idx0, ns, lane));
}
__device__ __forceinline__ u32 finish_granule(rsrc_t rs, int off, u32 tag, v2u g) {
#pragma unroll 1
  for (unsigned sp = 0; sp < SPIN_CAP; sp++) {
    if (__all(g.y == tag)) break;
    __builtin_amdgcn_s_sleep(1);
    g = ld8(rs, off);
  }
  __builtin_amdgcn_fence(__ATOMIC_ACQUIRE, "wavefront");
  return g.x;
}

template <int ROW> __device__ __forceinline__ void pqw4(u32 m0v, f2 a, f2 b, f2 c, f2 d) {
  const u32 h0 = pkh(a.x, a.y), h1 = pkh(b.x, b.y), h2_ = pkh(c.x, c.y), h3 = pkh(d.x, d.y);
  asm volatile("s_mov_b32 m0, %4\n\ts_nop 0\n\tds_write_addtid_b32 %0 offset:%c5\n\tds_write_addtid_b32 %1 offset:%c6\n\t"
               "ds_write_addtid_b32 %2 offset:%c7\n\tds_write_addtid_b32 %3 offset:%c8"
               ::"v"(h0), "v"(h1), "v"(h2_), "v"(h3), "s"(m0v), "i"(ROW * PQ_ROW), "i"((ROW + 1) * PQ_ROW),
               "i"((ROW + 2) * PQ_ROW), "i"((ROW + 3) * PQ_ROW) : "memory");
}
template <int ROW> __device__ __forceinline__ void pqw1(u32 m0v, f2 a) {
  const u32 h0 = pkh(a.x, a.y);
  asm volatile("s_mov_b32 m0, %1\n\ts_nop 0\n\tds_write_addtid_b32 %0 offset:%c2" ::"v"(h0), "s"(m0v), "i"(ROW * PQ_ROW) : "memory");
}
__device__ __forceinline__ f2 zpow(float al, float rv) {
  const float e = __builtin_amdgcn_exp2f(al);
  const float r = __builtin_amdgcn_fractf(rv);
  return f2{e * __builtin_amdgcn_cosf(r), e * __builtin_amdgcn_sinf(r)};
}
__device__ __forceinline__ float pow2b(int e) { return asf((u32)e << 23); }

__device__ __forceinline__ void ln1_rows(LAS unsigned char *S, v4u x0, v4u x1, int w, int lane) {
  constexpr u32 ONES = 0x3F803F80u;
  float v[4];
  v[0] = dot2bf(x0.w, ONES, dot2bf(x0.z, ONES, dot2bf(x0.y, ONES, dot2bf(x0.x, ONES, 0.f))));
  v[1] = dot2bf(x0.w, x0.w, dot2bf(x0.z, x0.z, dot2bf(x0.y, x0.y, dot2bf(x0.x, x0.x, 0.f))));
  v[2] = dot2bf(x1.w, ONES, dot2bf(x1.z, ONES, dot2bf(x1.y, ONES, dot2bf(x1.x, ONES, 0.f))));
  v[3] = dot2bf(x1.w, x1.w, dot2bf(x1.z, x1.z, dot2bf(x1.y, x1.y, dot2bf(x1.x, x1.x, 0.f))));
  if (lane < 56) {
    *(LAS v4u *)(S + LD_XROW + (2 * w) * 960 + lane * 16) = x0;
    *(LAS v4u *)(S + LD_XROW + (2 * w + 1) * 960 + lane * 16) = x1;
  }
#pragma unroll
  for (int i = 0; i < 4; i++) v[i] = lane < 56 ? v[i] : 0.f;
#pragma unroll
  for (int i = 0; i < 4; i++) v[i] += dpp0<0x111, 0xF>(v[i]);
#pragma unroll
  for (int i = 0; i < 4; i++) v[i] += dpp0<0x112, 0xF>(v[i]);
#pragma unroll
  for (int i = 0; i < 4; i++) v[i] += dpp0<0x114, 0xF>(v[i]);
#pragma unroll
  for (int i = 0; i < 4; i++) v[i] += dpp0<0x118, 0xF>(v[i]);
#pragma unroll
  for (int i = 0; i < 4; i++) v[i] += dpp0<0x142, 0xA>(v[i]);
#pragma unroll
  for (int i = 0; i < 4; i++) v[i] += dpp0<0x143, 0xC>(v[i]);
  const float s1 = asf(__builtin_amdgcn_readlane(__builtin_bit_cast(int, v[2]), 63));
  const float q1 = asf(__builtin_amdgcn_readlane(__builtin_bit_cast(int, v[3]), 63));
  const float s = lane == 62 ? s1 : v[0], q = lane == 62 ? q1 : v[1];
  const float mean = s * (1.0f / D);
  const float var = rbf(fmaxf(q * (1.0f / D) - mean * mean, 0.f));
  const float sd = rbf(__builtin_amdgcn_sqrtf(rbf(var + 1e-5f)));
  if (lane >= 62) *(LAS u32 *)(S + LD_STAT + (2 * w + 63 - lane) * 4) = pkbf(mean, sd);
}
__device__ __forceinline__ void publish_xt(LAS unsigned char *S, const rsrc_t rs, int lane, int w, int tau, u32 tag) {
  const int G = lane >> 4, e = (lane >> 2) & 3, pp = lane & 3;
  LAS unsigned char *ax = S + LD_XROW + (4 * G + e) * 960 + 4 * pp * 2 + w * 32;
  const int go = WS_XG + tau * (224 * 128) + w * 1024 + (lane & 15) * 64 + G * 16;
  const v2u v0 = trread(ax), v1 = trread(ax + 256), v2 = trread(ax + 512), v3 = trread(ax + (w < 4 ? 768 : 512));
  st16(rs, go, v4u{v0.x, tag, v0.y, tag});
  st16(rs, go + 8192, v4u{v1.x, tag, v1.y, tag});
  st16(rs, go + 16384, v4u{v2.x, tag, v2.y, tag});
  if (w < 4) st16(rs, go + 24576, v4u{v3.x, tag, v3.y, tag});
  if (w == 7 && lane < 16) st8(rs, WS_STAT1 + (16 * tau + lane) * 8, v2u{*(LAS u32 *)(S + LD_STAT + lane * 4), tag});
}

__device__ __forceinline__ void main_role(const Params &p, LAS unsigned char *S, const rsrc_t rs, const int tid,
                                          const int lane, const int w, const int b, const u32 epoch) {
  const int g = b & 7, r = b >> 3, m = 28 * g + r;
  const int cch = w >> 2, t8 = tid & 255, dch = 2 * m + cch;
  const int lane16 = lane * 16;
  const bool xprod = (r & 1) == 0;
  const int tau = 16 * g + (r >> 1), mi = 14 * g + (r >> 1);
  const u32 tm = tid < 504 ? tid : 503, mrow = (tm * 21846u) >> 16, mpc = tm - 3 * mrow;
  v4u rl[3];
  u32 cv[3];
  {
    const void *rb = xprod ? (const void *)p.x : (const void *)p.wmod;
    const u32 lo = xprod ? (u32)(lane < 56 ? lane : 55) * 16u : mrow * (12u * D) + mpc * 16u;
    const u32 u0 = xprod ? (u32)(16 * tau + 2 * w) * (2u * D) : (u32)mi * 48u;
    const u32 st = xprod ? 2u * D : 168u * 12u * D;
    const bool v2 = xprod || mrow < 112;
    rl[0] = gld<v4u>(rb, lo + u0);
    rl[1] = gld<v4u>(rb, lo + u0 + st);
    rl[2] = gld<v4u>(rb, lo + u0 + (xprod ? st : (v2 ? 2 * st : 0u)));
    cv[0] = gld<u16>(p.cvec, mrow * 2u);
    cv[1] = gld<u16>(p.cvec, mrow * 2u + 336u);
    cv[2] = gld<u16>(p.cvec, (mrow < 112 ? mrow : 111u) * 2u + 672u);
  }
  PIN();
  const u32 po = (u32)(dch * NS + t8) * 2u;
  const u32 pAre = gld<u16>(p.Are, po), pAim = gld<u16>(p.Aim, po), pBre = gld<u16>(p.Bre, po), pBim = gld<u16>(p.Bim, po);
  const u32 pCre = gld<u16>(p.Cre, po), pCim = gld<u16>(p.Cim, po);
  const u32 sLdt = *(const __attribute__((address_space(4))) u32 *)(p.logdt + 2 * m);
  const u32 sDsk = *(const __attribute__((address_space(4))) u32 *)(p.Dsk + 2 * m);
  const u32 pLdt = cch ? sLdt >> 16 : sLdt & 0xFFFFu, pDsk = cch ? sDsk >> 16 : sDsk & 0xFFFFu;
  const u32 tc = tid < 448 ? tid : 447, srow = (tc * 2341u) >> 16, scol = tc - 28 * srow;
  const v4u ws0 = gld<v4u>(p.wg, srow * (2u * F) + scol * 16u + (u32)(16 * (m >> 3) * F + 224 * (m & 7)) * 2u);
  const v4u ws1 = gld<v4u>(p.wu, srow * (2u * F) + scol * 16u + (u32)(16 * (m >> 3) * F + 224 * (m & 7)) * 2u);
  const v4u ws2 = gld<v4u>(p.wd, srow * (2u * D) + scol * 16u + (u32)(16 * (m >> 1) * D + 224 * (m & 1)) * 2u);
  PIN();
  asm volatile("" : "+v"(rl[0]), "+v"(rl[1]), "+v"(rl[2]));
  const u32 tag = __builtin_amdgcn_readfirstlane(epoch) + 1u;

  if (xprod) {
    ln1_rows(S, rl[0], rl[1], w, lane);
  } else {
    f2 a[4] = {f2{0.f, 0.f}, f2{0.f, 0.f}, f2{0.f, 0.f}, f2{0.f, 0.f}};
#pragma unroll
    for (int i = 0; i < 3; i++) {
      const float cx = bf2f(cv[i]);
      float sl = rbf(cx * __builtin_amdgcn_rcpf(1.0f + __builtin_amdgcn_exp2f(cx * -1.44269504088896f)));
      if (i == 2) sl = mrow < 112 ? sl : 0.f;
      const f2 s2 = {sl, sl};
      a[0] = __builtin_elementwise_fma(s2, f2{bflo(rl[i].x), bfhi(rl[i].x)}, a[0]);
      a[1] = __builtin_elementwise_fma(s2, f2{bflo(rl[i].y), bfhi(rl[i].y)}, a[1]);
      a[2] = __builtin_elementwise_fma(s2, f2{bflo(rl[i].z), bfhi(rl[i].z)}, a[2]);
      a[3] = __builtin_elementwise_fma(s2, f2{bflo(rl[i].w), bfhi(rl[i].w)}, a[3]);
    }
    if (tid < 504) {
      LAS f32x4 *mp = (LAS f32x4 *)(S + LD_XROW + tm * 32);
      mp[0] = f32x4{a[0].x, a[0].y, a[1].x, a[1].y}; mp[1] = f32x4{a[2].x, a[2].y, a[3].x, a[3].y};
    }
  }
  if (tid < 448) {
    LAS unsigned char *wp = S + LD_WSTG + srow * WSTG_ROW + scol * 16;
    *(LAS v4u *)(wp) = ws0;
    *(LAS v4u *)(wp + WSTG_M) = ws1;
    *(LAS v4u *)(wp + 2 * WSTG_M) = ws2;
  }
  asm volatile("" ::"v"(ws0), "v"(ws1), "v"(ws2), "v"(pAre), "v"(pAim), "v"(pBre), "v"(pBim), "v"(pCre), "v"(pCim));
  STAMP(1);
  BAR();
  if (xprod) {
    publish_xt(S, rs, lane, w, tau, tag);
  } else if (w < 6) {
    const int col = tid >> 4, part = tid & 15;
    LAS float *mp = (LAS float *)(S + LD_XROW) + part * 24 + col;
    float sm = part < 8 ? mp[160 * 24] : 0.f;
#pragma unroll
    for (int i = 0; i < 10; i++) sm += mp[i * 16 * 24];
    sm += dpp0<0x111, 0xF>(sm); sm += dpp0<0x112, 0xF>(sm); sm += dpp0<0x114, 0xF>(sm); sm += dpp0<0x118, 0xF>(sm);
    if (part == 15) st8(rs, WS_MODG + (24 * mi + col) * 8, v2u{__builtin_bit_cast(u32, rbf(sm)), tag});
  }
  PIN();
  STAMP(2);

  const float dt = rbf(__builtin_amdgcn_exp2f(bf2f(pLdt) * 1.44269504088896f));
  const float al = (dt * bf2f(pAre)) * 1.44269504088896f;
  const float rv = (dt * bf2f(pAim)) * 0.159154943091895f;
  const f2 Wn = cmul(f2{bf2f(pCre), -bf2f(pCim)}, f2{dt * bf2f(pBre), -dt * bf2f(pBim)});
  {
    const float mx = wave_max_nonneg(fmaxf(fabsf(Wn.x), fabsf(Wn.y)));
    if (lane == 0) *(LAS float *)(S + LD_WMAX + w * 4) = mx;
  }
  const u32 m0v = (u32)(LD_PQ + (w >> 2) * PQ_CH + (w & 3) * 256);
  const f2 p1 = zpow(al, rv), z8 = zpow(al * 8.f, rv * -8.f), Z1 = zpow(al * 128.f, rv * 128.f);
  const f2 p2 = cmul(p1, p1), p3 = cmul(p2, p1), p4 = cmul(p2, p2), p5 = cmul(p4, p1), p6 = cmul(p4, p2), p7 = cmul(p4, p3);
  pqw4<0>(m0v, f2{1.f, 0.f}, p1, p2, p3);
  pqw4<4>(m0v, p4, p5, p6, p7);
  pqw4<8>(m0v, Z1, cmul(Z1, p1), cmul(Z1, p2), cmul(Z1, p3));
  pqw4<12>(m0v, cmul(Z1, p4), cmul(Z1, p5), cmul(Z1, p6), cmul(Z1, p7));
  PIN();
  v4u wdfrag;
  {
    const int G = lane >> 4, e = (lane >> 2) & 3, pp = lane & 3, h = G >> 1;
    LAS unsigned char *a0 = S + LD_WSTG + (G & 1) * WSTG_M + (8 * h + e) * WSTG_ROW + (16 * w + 4 * pp) * 2;
    LAS unsigned char *a1 = a0 + (w < 6 ? 256 : 0);
    LAS unsigned char *a2 = S + LD_WSTG + 2 * WSTG_M + (4 * h + e) * WSTG_ROW + (32 * (w < 7 ? w : 6) + 16 * (G & 1) + 4 * pp) * 2;
    const v4u v0 = tr8(a0, a0 + 4 * WSTG_ROW), v1 = tr8(a1, a1 + 4 * WSTG_ROW);
    wdfrag = tr8(a2, a2 + 8 * WSTG_ROW);
    const int o1 = WS_W1F + ((14 * (m & 7) + w) * 28 + (m >> 3)) * 1024 + lane16;
    st16(rs, o1, v0);
    if (w < 6) st16(rs, o1 + 8 * 28 * 1024, v1);
    if (w == 7 && lane < 2 && m + 224 * lane < NCNT) st4(rs, WS_CNT + (m + 224 * lane) * 256, 0u);
  }
  PIN();
  STAMP(3);
  v4u xg[2], sg[2], mg[4], kt0;
  v2u kt1;
  int ebW;
  const int hch = (tid >> 2) & 1, l4 = 16 * (tid >> 3) + 4 * (tid & 3);
  const int xo = ((tid >> 3) * 224) * 128 + (tid & 7) * 16;
  const int xu = WS_XG + m * 128, mo = WS_MODG + 2 * m * 8;
#define LD_HANDOFF_A() do { \
    sg[0] = ld16s(rs, tid * 16, WS_STAT1); sg[1] = ld16s(rs, tid * 16, WS_STAT1 + 8192); \
    xg[0] = ld16s(rs, xo, xu); xg[1] = ld16s(rs, xo, xu + 64 * 224 * 128); \
    mg[0] = ld16s(rs, 0, mo + 0 * D * 8); mg[1] = ld16s(rs, 0, mo + 1 * D * 8); \
    mg[2] = ld16s(rs, 0, mo + 2 * D * 8); mg[3] = ld16s(rs, 0, mo + 4 * D * 8); } while (0)
  {
    const f2 Z2 = cmul(Z1, Z1), Z3 = cmul(Z2, Z1), Z4 = cmul(Z2, Z2);
    pqw4<16>(m0v, Z2, cmul(Z2, p2), cmul(Z2, p4), cmul(Z2, p6));
    pqw4<20>(m0v, Z3, cmul(Z3, p2), cmul(Z3, p4), cmul(Z3, p6));
    const f2 Z5 = cmul(Z4, Z1), Z6 = cmul(Z4, Z2), Z7 = cmul(Z4, Z3), Z8 = cmul(Z4, Z4);
    pqw4<24>(m0v, Z4, cmul(Z4, p4), Z5, cmul(Z5, p4));
    pqw4<28>(m0v, Z6, cmul(Z6, p4), Z7, cmul(Z7, p4));
    pqw4<32>(m0v, Z8, cmul(Z8, Z1), cmul(Z8, Z2), cmul(Z8, Z3));
    pqw4<36>(m0v, cmul(Z8, Z4), cmul(Z8, Z5), cmul(Z8, Z6), cmul(Z8, Z7));
    pqw1<40>(m0v, cmul(Z8, Z8));
    STAMP(20);
    kt0 = gld<v4u>(&g_ktab, (u32)t8 * 32u);
    kt1 = gld<v2u>(&g_ktab, (u32)t8 * 32u + 16u);
    LD_HANDOFF_A();
    PIN();
    asm volatile("s_waitcnt vmcnt(10) ; PUBWAIT" ::: "memory");
    BAR();
    if (tid == 0) st4(rs, WS_WRDY + m * 16, tag);
    STAMP(21);
    {
      const f32x4 mx4 = *(LAS f32x4 *)(S + LD_WMAX + (w & 4) * 4);
      const float mx = fmaxf(fmaxf(mx4[0], mx4[1]), fmaxf(mx4[2], mx4[3]));
      ebW = (int)((__builtin_bit_cast(u32, mx) >> 23) & 0xFF);
      ebW = ebW < 14 ? 14 : (ebW > 253 ? 253 : ebW);
    }
    const f2 z16 = cmul(z8, z8), z24 = cmul(z16, z8), z32 = cmul(z16, z16);
    f2 q0 = Wn * pow2b(267 - ebW), q1 = cmul(q0, z8), q2 = cmul(q0, z16), q3 = cmul(q0, z24);
    pqw4<41>(m0v, q0, q1, q2, q3);
    q0 = cmul(q0, z32); q1 = cmul(q1, z32); q2 = cmul(q2, z32); q3 = cmul(q3, z32);
    pqw4<45>(m0v, q0, q1, q2, q3);
    q0 = cmul(q0, z32); q1 = cmul(q1, z32); q2 = cmul(q2, z32); q3 = cmul(q3, z32);
    pqw4<49>(m0v, q0, q1, q2, q3);
    q0 = cmul(q0, z32); q1 = cmul(q1, z32); q2 = cmul(q2, z32); q3 = cmul(q3, z32);
    pqw4<53>(m0v, q0, q1, q2, q3);
  }
  STAMP(4);
  BAR();
  {
    const int tile = w & 3;
    if (tile < 3) {
      LAS unsigned char *pa = S + LD_PQ + cch * PQ_CH + (16 * tile + (lane & 15)) * PQ_ROW + (lane >> 4) * 16;
      LAS unsigned char *pb = S + LD_PQ + cch * PQ_CH + (41 + (lane & 15)) * PQ_ROW + (lane >> 4) * 16;
      f32x4 acc0 = {0.f, 0.f, 0.f, 0.f}, acc1 = acc0;
      v4u fa[16], fb[16];
#pragma unroll
      for (int ks = 0; ks < 16; ks++) { fa[ks] = *(LAS v4u *)(pa + ks * 64); fb[ks] = *(LAS v4u *)(pb + ks * 64); }
      PIN();
#pragma unroll
      for (int ks = 0; ks < 16; ks += 2) { acc0 = mfma16h(fa[ks], fb[ks], acc0); acc1 = mfma16h(fa[ks + 1], fb[ks + 1], acc1); }
      acc0 = (acc0 + acc1) * (1.0f / 256.0f);
      LAS u16 *kd = (LAS u16 *)(S + LD_KD + cch * KD_CH) + (16 * tile + 4 * (lane >> 4)) * 16 + (lane & 15);
      const u32 k01 = pkh(acc0[0], acc0[1]), k23 = pkh(acc0[2], acc0[3]);
      kd[0] = (u16)k01; kd[16] = (u16)(k01 >> 16); kd[32] = (u16)k23; kd[48] = (u16)(k23 >> 16);
    } else if (lane < 16) {
      ((LAS u16 *)(S + LD_KD + cch * KD_CH))[768 + lane] = 0;
    }
  }
#pragma unroll 1
  for (unsigned sp = 0; sp < SPIN_CAP; sp++) {
    const u32 d = ((xg[0].y ^ tag) | (xg[0].w ^ tag) | (xg[1].y ^ tag) | (xg[1].w ^ tag)) |
                  ((sg[0].y ^ tag) | (sg[0].w ^ tag) | (sg[1].y ^ tag) | (sg[1].w ^ tag)) |
                  ((mg[0].y ^ tag) | (mg[0].w ^ tag) | (mg[1].y ^ tag) | (mg[1].w ^ tag)) |
                  ((mg[2].y ^ tag) | (mg[2].w ^ tag) | (mg[3].y ^ tag) | (mg[3].w ^ tag));
    if (__all(d == 0)) break;
    __builtin_amdgcn_s_sleep(1);
    LD_HANDOFF_A();
  }
#undef LD_HANDOFF_A
  __builtin_amdgcn_fence(__ATOMIC_ACQUIRE, "wavefront");
  *(LAS f32x4 *)(S + LD_ST1 + tid * 16) = f32x4{bflo(sg[0].x), __builtin_amdgcn_rcpf(bfhi(sg[0].x)), bflo(sg[0].z), __builtin_amdgcn_rcpf(bfhi(sg[0].z))};
  *(LAS f32x4 *)(S + LD_ST1 + 8192 + tid * 16) = f32x4{bflo(sg[1].x), __builtin_amdgcn_rcpf(bfhi(sg[1].x)), bflo(sg[1].z), __builtin_amdgcn_rcpf(bfhi(sg[1].z))};
  STAMP(5);
  BAR();
  const int fo = WS_WRDY + t8 * 16;
  u32 wr = __builtin_amdgcn_raw_buffer_load_b32(rs, fo, 0, SC1);
  PIN();

  {
    LAS unsigned char *kd = S + LD_KD + cch * KD_CH;
    u32 v[11];
    const u32 ix[6] = {kt0.x, kt0.y, kt0.z, kt0.w, kt1.x, kt1.y};
#pragma unroll
    for (int i = 0; i < 11; i++) v[i] = *(LAS u16 *)(kd + ((i & 1) ? (ix[i >> 1] >> 16) : (ix[i >> 1] & 0xFFFFu)));
    LAS unsigned char *kc = S + LD_KC + cch * KC_CH + (16 + 8 * t8) * 2;
    u32 pr[10];
#pragma unroll
    for (int i = 0; i < 10; i++) pr[i] = v[i] | (v[i + 1] << 16);
#pragma unroll
    for (int e = 0; e < 4; e++) *(LAS v4u *)(kc + e * KC_CP) = v4u{pr[3 - e], pr[5 - e], pr[7 - e], pr[9 - e]};
    if (t8 < 8) *(LAS v4u *)(S + LD_KC + cch * KC_CH + (t8 >> 1) * KC_CP + (t8 & 1) * 16) = v4u{0, 0, 0, 0};
  }
  float A1, A2, c2;
  {
    const float sah = asf(hch ? mg[0].z : mg[0].x), c1h = rbf(1.0f + asf(hch ? mg[1].z : mg[1].x));
    int eh = (int)((__builtin_bit_cast(u32, 21.5f * fabsf(c1h) + fabsf(sah)) >> 23) & 0xFF);
    eh = eh < 14 ? 14 : (eh > 253 ? 253 : eh);
    const float sH = pow2b(267 - eh);
    const f2 c1s = {c1h * sH, c1h * sH}, sas = {sah * sH, sah * sH};
#pragma unroll
    for (int i = 0; i < 2; i++) {
      const u32 x01 = xg[i].x, x23 = xg[i].z;
      const f32x4 sa_ = *(LAS f32x4 *)(S + LD_ST1 + (l4 + 1024 * i) * 8), sb_ = *(LAS f32x4 *)(S + LD_ST1 + (l4 + 1024 * i) * 8 + 16);
      const f2 h01 = __builtin_elementwise_fma((f2{bflo(x01), bfhi(x01)} - f2{sa_[0], sa_[2]}) * f2{sa_[1], sa_[3]}, c1s, sas);
      const f2 h23 = __builtin_elementwise_fma((f2{bflo(x23), bfhi(x23)} - f2{sb_[0], sb_[2]}) * f2{sb_[1], sb_[3]}, c1s, sas);
      *(LAS v2u *)(S + LD_U + hch * U_CH + (256 + l4 + 1024 * i) * 2) = v2u{pkh(h01.x, h01.y), pkh(h23.x, h23.y)};
      *(LAS v2u *)(S + LD_XS + hch * 4096 + (l4 + 1024 * i) * 2) = v2u{x01, x23};
    }
    if (t8 < 32) *(LAS v4u *)(S + LD_U + cch * U_CH + 16 * t8) = v4u{0, 0, 0, 0};
    const float sac = asf(cch ? mg[0].z : mg[0].x), c1c = rbf(1.0f + asf(cch ? mg[1].z : mg[1].x));
    int ec = (int)((__builtin_bit_cast(u32, 21.5f * fabsf(c1c) + fabsf(sac)) >> 23) & 0xFF);
    ec = ec < 14 ? 14 : (ec > 253 ? 253 : ec);
    const float ga = asf(cch ? mg[2].z : mg[2].x);
    const float iH = pow2b(ec - 13);
    A1 = ga * iH * pow2b(ebW - 5);
    A2 = ga * bf2f(pDsk) * iH;
    c2 = rbf(1.0f + asf(cch ? mg[3].z : mg[3].x));
  }
#pragma unroll 1
  for (unsigned sp = 0; sp < SPIN_CAP; sp++) {
    if (__all(wr == tag)) break;
    __builtin_amdgcn_s_sleep(1);
    wr = __builtin_amdgcn_raw_buffer_load_b32(rs, fo, 0, SC1);
  }
  __builtin_amdgcn_fence(__ATOMIC_ACQUIRE, "wavefront");
  STAMP(6);
  BAR();
  if (b == 0 && tid == 64) st4(rs, WS_EPOCH, tag);
  if (w < 7) st16(rs, WS_WDF + ((7 * (m & 1) + w) * 112 + (m >> 1)) * 1024 + lane16, wdfrag);
  PIN();

  const int fc = 7 * (g & 1) + r % 7, tt = 4 * (g >> 1) + r / 7;
  const int phi = 8 * fc + w;
  const int dtile = r % 7, t2 = 4 * g + r / 7;
  v4u w1[28];
  const int w1o = WS_W1F + phi * 28 * 1024;
#define LDW1(s) do { w1[s] = ld16s(rs, lane16, w1o + (s) * 1024); PIN(); } while (0)
  v2u csg_;
  const int cso = WS_CSB + (phi * 64 + lane) * 8;

  {
    const int c = cch, pq = w & 3;
    const int a15 = lane & 15, q = lane >> 4, e = (lane >> 2) & 3, pp = lane & 3;
    LAS unsigned char *ka = S + LD_KC + c * KC_CH + e * KC_CP + (16 + 16 * (q >> 1) - 8 * (q & 1) + 4 * pp) * 2 + pq * 128 - 8;
    LAS unsigned char *ub = S + LD_U + c * U_CH + 512 + 32 * a15 - 32 * (q >> 1) + 16 * (q & 1) - pq * 128 - 64;
    const int l16 = 16 * a15 + 4 * q;
    const int xoff = ((q & 1) ? WS_X1S : WS_X1T) + ((a15 >> 2) * D + dch) * 128 + (16 * (a15 & 3) + 8 * (q >> 1)) * 2;
#define RD_A(i) tr8(ka + (i) * 512 + pi * 64 + 8, ka + (i) * 512 + pi * 64)
#define RD_B(i) (*(LAS v4u *)(ub + (i) * 512 + 64 - pi * 64))
#pragma unroll
    for (int hf = 0; hf < 2; hf++) {
      LAS unsigned char *ex = S + LD_EX + hf * 32768 + c * 16384 + lane16;
      f32x4 acc[4];
#pragma unroll
      for (int i = 0; i < 4; i++) acc[i] = f32x4{0.f, 0.f, 0.f, 0.f};
#pragma unroll
      for (int pi = 0; pi < 2; pi++) {
        v4u A[8], B[8];
        if (hf == 0) {
          A[0] = RD_A(0); B[0] = RD_B(0); B[1] = RD_B(1); B[2] = RD_B(2); B[3] = RD_B(3);
          A[1] = RD_A(1); A[2] = RD_A(2); A[3] = RD_A(3);
        } else {
          A[0] = RD_A(0); B[4] = RD_B(4); B[5] = RD_B(5); B[6] = RD_B(6); B[7] = RD_B(7);
          A[1] = RD_A(1); B[3] = RD_B(3); A[2] = RD_A(2); B[2] = RD_B(2); A[3] = RD_A(3); B[1] = RD_B(1);
          A[4] = RD_A(4); B[0] = RD_B(0); A[5] = RD_A(5); A[6] = RD_A(6); A[7] = RD_A(7);
        }
        PIN();
#pragma unroll
        for (int qq = 0; qq < 8; qq++) {
#pragma unroll
          for (int tl = 0; tl < 4; tl++)
            if (qq <= 4 * hf + tl) acc[tl] = mfma16h(A[qq], B[4 * hf + tl - qq], acc[tl]);
          PIN();
          if (hf == 0 && qq < 2) LDW1(2 * pi + qq);
          if (hf == 1 && qq < 7) LDW1(4 + 7 * pi + qq);
        }
      }
      STAMP(17 + 2 * hf);
#pragma unroll
      for (int tl = 0; tl < 4; tl++) *(LAS f32x4 *)(ex + (pq * 4 + tl) * 1024) = acc[tl];
      if (hf == 1) asm volatile("s_waitcnt vmcnt(14) ; PUBWAIT" ::: "memory");
      BAR();
      if (hf == 1) {
        if (tid < 8) __hip_atomic_fetch_add((gu32 *)(p.ws + WS_CNT) + (0 * 128 + tid * 16 + (m & 15)) * 64, 1u, RLX_AGENT);
        csg_ = ld8(rs, cso);
      }
      const int t = 4 * hf + pq;
      const int l0 = 256 * t + l16;
      const f32x4 y0 = *(LAS f32x4 *)(ex + (0 * 4 + pq) * 1024), y1 = *(LAS f32x4 *)(ex + (1 * 4 + pq) * 1024);
      const f32x4 y2 = *(LAS f32x4 *)(ex + (2 * 4 + pq) * 1024), y3 = *(LAS f32x4 *)(ex + (3 * 4 + pq) * 1024);
      const v2u hh = *(LAS v2u *)(S + LD_U + c * U_CH + (256 + l0) * 2);
      const v2u xx = *(LAS v2u *)(S + LD_XS + c * 4096 + l0 * 2);
      const f32x4 y = (y0 + y1) + (y2 + y3);
      const h2 ha = ash2(hh.x), hb = ash2(hh.y);
      const f2 a1 = {A1, A1}, a2 = {A2, A2}, cc = {c2, c2};
      const f2 o01 = __builtin_elementwise_fma(a2, f2{(float)ha.x, (float)ha.y},
                         __builtin_elementwise_fma(a1, f2{y[0], y[1]}, f2{bflo(xx.x), bfhi(xx.x)}));
      const f2 o23 = __builtin_elementwise_fma(a2, f2{(float)hb.x, (float)hb.y},
                         __builtin_elementwise_fma(a1, f2{y[2], y[3]}, f2{bflo(xx.y), bfhi(xx.y)}));
      const u32 X0 = pkbf(o01.x, o01.y), X1 = pkbf(o23.x, o23.y);
      const f2 s01 = f2{bflo(X0), bfhi(X0)} * cc, s23 = f2{bflo(X1), bfhi(X1)} * cc;
      const u32 Y0 = pkbf(s01.x, s01.y), Y1 = pkbf(s23.x, s23.y);
      const auto r0 = __builtin_amdgcn_permlane16_swap(X0, Y0, false, false);
      const auto r1 = __builtin_amdgcn_permlane16_swap(X1, Y1, false, false);
      st16(rs, xoff + 4 * t * D * 128, v4u{r0[0], r1[0], r0[1], r1[1]});
      PIN();
      STAMP(18 + 4 * hf);
    }
#pragma unroll
    for (int i = 18; i < 24; i++) LDW1(i);
    asm volatile("s_waitcnt vmcnt(6) ; PUBWAIT" ::: "memory");
    BAR();
    if (tid < 8) __hip_atomic_fetch_add((gu32 *)(p.ws + WS_CNT) + (1 * 128 + tid * 16 + (m & 15)) * 64, 1u, RLX_AGENT);
#undef RD_A
#undef RD_B
  }
  STAMP(7);

  if (w == 0) {
    poll_counters(p, 0 * 128 + g * 16, 16, 14u, lane);
  } else {
#pragma unroll
    for (int i = 24; i < 28; i++) LDW1(i);
  }
  BAR();
  STAMP(8);
  v4u xs[7];
  const int xso = tid * 16;
  const int xlo = (tid >> 3) * 128 + (((tid & 7) ^ (((tid >> 4) & 1) << 2)) * 16);
#pragma unroll
  for (int i = 0; i < 7; i++) xs[i] = ld16s(rs, xso, WS_X1S + tt * XB_SZ + i * 8192);
  PIN();
  if (w == 0) {
#pragma unroll
    for (int i = 24; i < 28; i++) LDW1(i);
  }
  *(LAS u32 *)(S + LD_CSL + (w * 64 + lane) * 4) = finish_granule(rs, cso, tag, csg_);
#define NWA 4
#define NWB 4
  constexpr int NWPF = NWA + NWB;
  v4u wd[28];
  const int wdo = WS_WDF + (2 * dtile * 112 + 14 * w) * 1024;
#define LDWD(i) (wd[i] = ld16s(rs, lane16, wdo + (((i) / 14) * 112 + (i) % 14) * 1024))
#pragma unroll
  for (int i = 0; i < 7; i++) {
    *(LAS v4u *)(S + LD_XB + xlo + i * 8192) = xs[i];
    if (i < NWA) LDWD(i);
    PIN();
  }
  BAR();
  STAMP(9);
  v2u sg2[2];
  u32 pk;
  {
    const int G = lane >> 4, e = (lane >> 2) & 3, pp = lane & 3, h = lane >> 5;
    const int sw = ((e >> 1) & 1) * 64;
    LAS unsigned char *bx0 = S + LD_XB + (8 * h + e) * 128 + ((0 + 32 * (G & 1) + 8 * pp) ^ sw);
    LAS unsigned char *bx1 = S + LD_XB + (8 * h + e) * 128 + ((64 + 32 * (G & 1) + 8 * pp) ^ sw);
    LAS unsigned char *cl = S + LD_CSL + w * 256 + 16 * h;
    const int ho = WS_HF + phi * 1024 + lane16;
    f32x16 acc[2][2];
    f2 ep1[2], ep2[2];
    u32 hp[2][4];
#define EPI_PART(hf_, j, gq) do { \
      const f32x4 csg = *(LAS f32x4 *)(cl + 32 * (gq)), csu = *(LAS f32x4 *)(cl + 32 * (gq) + 64); \
      const f32x4 bg = *(LAS f32x4 *)(cl + 32 * (gq) + 128), bu = *(LAS f32x4 *)(cl + 32 * (gq) + 192); \
      _Pragma("unroll") for (int i = 0; i < 4; i += 2) { \
        const f2 gg = __builtin_elementwise_fma(ep1[j], f2{acc[hf_][j][4 * (gq) + i], acc[hf_][j][4 * (gq) + i + 1]}, \
                          __builtin_elementwise_fma(ep2[j], f2{csg[i], csg[i + 1]}, f2{bg[i], bg[i + 1]})); \
        const f2 uu = __builtin_elementwise_fma(ep1[j], f2{acc[hf_][j][8 + 4 * (gq) + i], acc[hf_][j][8 + 4 * (gq) + i + 1]}, \
                          __builtin_elementwise_fma(ep2[j], f2{csu[i], csu[i + 1]}, f2{bu[i], bu[i + 1]})); \
        const f2 hv = gg * uu; \
        hp[j][2 * (gq) + i / 2] = pkbf(hv.x, hv.y); \
      } } while (0)
#define EPI_STORE(hf_, j) st16(rs, ho + (2 * (16 * (hf_) + tt) + (j)) * 112 * 1024, v4u{hp[j][0], hp[j][1], hp[j][2], hp[j][3]})
#define EPI_STATS(hf_) do { \
      const int sto_ = WS_STAT2 + (64 * (16 * (hf_) + tt) + (lane & 31)) * 8; \
      const u32 sv0 = finish_granule(rs, sto_, tag, sg2[0]), sv1 = finish_granule(rs, sto_ + 256, tag, sg2[1]); \
      const float r0_ = __builtin_amdgcn_rcpf(bfhi(sv0)), r1_ = __builtin_amdgcn_rcpf(bfhi(sv1)); \
      ep1[0] = f2{r0_, r0_}; ep1[1] = f2{r1_, r1_}; \
      ep2[0] = f2{-r0_ * bflo(sv0), -r0_ * bflo(sv0)}; ep2[1] = f2{-r1_ * bflo(sv1), -r1_ * bflo(sv1)}; } while (0)
    constexpr int NREM = 28 - NWPF;
#define WDSTEP(s) (((s) * NREM / 27 != ((s) + 1) * NREM / 27 && (s) < 27) ? NWPF + (s) * NREM / 27 : -1)
    constexpr int PUBSTEP = 10, PUBCNT = (PUBSTEP + 1) * NREM / 27;
#pragma unroll
    for (int hf = 0; hf < 2; hf++) {
#pragma unroll
      for (int j = 0; j < 2; j++)
#pragma unroll
        for (int i = 0; i < 16; i++) acc[hf][j][i] = 0.f;
      v4u fb0[3], fb1[3];
      LAS unsigned char *bh0 = bx0 + hf * XB_SZ, *bh1 = bx1 + hf * XB_SZ;
      asm volatile("" : "+v"(bh0), "+v"(bh1));
#define RD_X(s) do { const int o_ = (s) * 2048; fb0[(s) % 3] = tr8(bh0 + o_, bh0 + o_ + 512); fb1[(s) % 3] = tr8(bh1 + o_, bh1 + o_ + 512); } while (0)
      RD_X(0); RD_X(1);
#pragma unroll
      for (int s = 0; s < 28; s++) {
        if (s + 2 < 28) RD_X(s + 2);
        if (hf == 0) {
          if ((s & 1) == 0 && s / 2 < NWB) LDWD(NWA + s / 2);
          if (s == 3 && w == 0) pk = peek_counters(p, 1 * 128 + g * 16, 16, lane);
          if (s == 9) {
            if (w == 0) finish_counters(p, 1 * 128 + g * 16, 16, 14u, lane, pk);
            BAR();
          }
          if (s >= 9 && s < 16) xs[s - 9] = ld16s(rs, xso, WS_X1S + (16 + tt) * XB_SZ + (s - 9) * 8192);
          if (s >= 21) *(LAS v4u *)(S + LD_XB + XB_SZ + xlo + (s - 21) * 8192) = xs[s - 21];
        } else {
          if (WDSTEP(s) >= 0) LDWD(WDSTEP(s) < 0 ? 0 : WDSTEP(s));
          if (s == PUBSTEP) {
            asm volatile("s_waitcnt vmcnt(%0) ; PUBWAIT" ::"n"(PUBCNT) : "memory");
            BAR();
            if (tid == 0) __hip_atomic_fetch_add((gu32 *)(p.ws + WS_CNT) + (256 + tt) * 64, 1u, RLX_AGENT);
          }
        }
        if (s == 20) {
          const int sto = WS_STAT2 + (64 * (16 * hf + tt) + (lane & 31)) * 8;
          sg2[0] = ld8(rs, sto); sg2[1] = ld8(rs, sto + 256);
        }
        PIN();
        acc[hf][0] = mfma32b(w1[s], fb0[s % 3], acc[hf][0]);
        acc[hf][1] = mfma32b(w1[s], fb1[s % 3], acc[hf][1]);
        PIN();
        if (s == 3) STAMP(23 + 3 * hf);
        if (s == 27) STAMP(24 + 3 * hf);
      }
#undef RD_X
      if (hf == 0) {
        EPI_STATS(0);
        EPI_PART(0, 0, 0); EPI_PART(0, 0, 1); EPI_PART(0, 1, 0); EPI_PART(0, 1, 1);
        PIN();
        EPI_STORE(0, 0); EPI_STORE(0, 1);
        PIN();
        BAR();
        STAMP(10);
      }
    }
    EPI_STATS(1);
    STAMP(28);
    EPI_PART(1, 0, 0); EPI_PART(1, 0, 1); EPI_PART(1, 1, 0); EPI_PART(1, 1, 1);
    if (w == 0) pk = peek_counters(p, 256 + (t2 >> 1), 1, lane);
    PIN();
    EPI_STORE(1, 0); EPI_STORE(1, 1);
    PIN();
    STAMP(11);
#undef EPI_PART
#undef EPI_STORE
#undef EPI_STATS
  }

  if (w == 0) finish_counters(p, 256 + (t2 >> 1), 1, 14u, lane, pk);
  BAR();
  STAMP(12);
  v4u gbg[2], hb[14], res[2];
  const int gbo = WS_MODG + (5 * D + 64 * dtile + 4 * (2 * w + (lane >> 5))) * 8;
  const int reso = WS_X1T + ((t2 >> 1) * D + 64 * dtile + ((tid & 255) >> 2)) * 128 + (t2 & 1) * 64 + (tid & 3) * 16;
  const int hbo = WS_HF + (t2 * 112 + 14 * w) * 1024;
#pragma unroll
  for (int k = 0; k < 5; k++) hb[k] = ld16s(rs, lane16, hbo + k * 1024);
  PIN();
  asm volatile("s_waitcnt vmcnt(5) ; PUBWAIT" ::: "memory");
  BAR();
  if (tid == 0) __hip_atomic_fetch_add((gu32 *)(p.ws + WS_CNT) + (256 + 16 + tt) * 64, 1u, RLX_AGENT);
#pragma unroll
  for (int k = 5; k < 14; k++) hb[k] = ld16s(rs, lane16, hbo + k * 1024);
  res[0] = ld16s(rs, reso, 0); res[1] = ld16s(rs, reso, 16 * D * 128);
  gbg[0] = ld16(rs, gbo); gbg[1] = ld16(rs, gbo + 16);
  PIN();
#pragma unroll
  for (int hf = 0; hf < 2; hf++) {
    f32x16 acc[2];
#pragma unroll
    for (int j = 0; j < 2; j++)
#pragma unroll
      for (int i = 0; i < 16; i++) acc[j][i] = 0.f;
#pragma unroll
    for (int k = 0; k < 14; k++) {
      acc[0] = mfma32b(wd[k], hb[k], acc[0]);
      acc[1] = mfma32b(wd[14 + k], hb[k], acc[1]);
      if (hf == 0 && k == 9 && w == 0) { PIN(); pk = peek_counters(p, 256 + 16 + (t2 >> 1), 1, lane); PIN(); }
    }
    PIN();
    STAMP(29 + hf);
#define LDHL(k) do { if (hf == 0) { hb[k] = ld16s(rs, lane16, hbo + (32 * 112 + (k)) * 1024); PIN(); } } while (0)
    if (hf == 0) {
      if (w == 0) finish_counters(p, 256 + 16 + (t2 >> 1), 1, 14u, lane, pk);
      BAR();
    }
    {
      const int col = lane & 31, h = lane >> 5;
      LAS unsigned char *rd = S + LD_RED + w * RED_W + col * RED_ROW + h * 16;
#pragma unroll
      for (int i = 0; i < 8; i++) {
        const int dl = i >> 2, gq = i & 3;
        *(LAS f32x4 *)(rd + dl * 128 + gq * 32) = f32x4{acc[dl][4 * gq], acc[dl][4 * gq + 1], acc[dl][4 * gq + 2], acc[dl][4 * gq + 3]};
        PIN();
        LDHL(i);
      }
      if (tid < 256) *(LAS v4u *)(S + LD_RES + (tid >> 2) * 64 + (tid & 3) * 16) = res[hf];
    }
    if (hf == 0) {
#pragma unroll 1
      for (unsigned sp = 0; sp < SPIN_CAP; sp++) {
        if (__all(gbg[0].y == tag && gbg[0].w == tag && gbg[1].y == tag && gbg[1].w == tag)) break;
        gbg[0] = ld16(rs, gbo); gbg[1] = ld16(rs, gbo + 16);
      }
    }
    BAR();
    {
      const int tok = lane & 31, dq = 2 * w + (lane >> 5);
      f32x4 part[8];
#pragma unroll
      for (int ww = 0; ww < 8; ww++) part[ww] = *(LAS f32x4 *)(S + LD_RED + ww * RED_W + tok * RED_ROW + dq * 16);
      const v2u rr = trread(S + LD_RES + (4 * dq + ((lane >> 2) & 3)) * 64 + (16 * ((lane >> 4) & 1) + 4 * (lane & 3)) * 2);
      PIN();
      LDHL(8); LDHL(9);
      const f32x4 s0 = (part[0] + part[1]) + (part[2] + part[3]);
      PIN();
      LDHL(10); LDHL(11);
      const f32x4 sum = s0 + ((part[4] + part[5]) + (part[6] + part[7]));
      PIN();
      LDHL(12); LDHL(13);
      const u32 b01 = pkbf(sum[0], sum[1]), b23 = pkbf(sum[2], sum[3]);
      const u32 t01 = pkbf(asf(gbg[0].x) * bflo(b01), asf(gbg[0].z) * bfhi(b01));
      const u32 t23 = pkbf(asf(gbg[1].x) * bflo(b23), asf(gbg[1].z) * bfhi(b23));
      const float o0 = bflo(rr.x) + bflo(t01), o1 = bfhi(rr.x) + bfhi(t01);
      const float o2 = bflo(rr.y) + bflo(t23), o3 = bfhi(rr.y) + bfhi(t23);
      *(GAS v2u *)((GAS char *)p.out + (u32)((32 * (32 * hf + t2) + tok) * D + 64 * dtile + 4 * dq) * 2u) = v2u{pkbf(o0, o1), pkbf(o2, o3)};
    }
#undef LDHL
    STAMP(13 + 2 * hf);
    if (hf == 0) BAR();
  }
#undef LDW1
}

__device__ __forceinline__ void side_role(const Params &p, LAS unsigned char *S, const rsrc_t rs, const int tid,
                                          const int lane, const int w, const int b, const u32 epoch) {
  const int g = b & 7, r = b >> 3, sidx = 4 * g + r - 28;
  const int sc = sidx < 28 ? sidx : 27;
  const int lane16 = lane * 16;
  const int tau = 16 * g + (r >> 1);
  const u32 xo = (u32)(16 * tau + 2 * w) * (2u * D) + (u32)(lane < 56 ? lane : 55) * 16u;
  const v4u x0 = gld<v4u>(p.x, xo), x1 = gld<v4u>(p.x, xo + 2u * D);
  const u32 tag = __builtin_amdgcn_readfirstlane(epoch) + 1u;
  if (tid == 0) st4(rs, WS_WRDY + (224 + sidx) * 16, tag);
  if ((r & 1) == 0) {
    ln1_rows(S, x0, x1, w, lane);
    STAMP(1);
    BAR();
    publish_xt(S, rs, lane, w, tau, tag);
  }
  STAMP(3);
  {
    const int dd = tid < D ? tid : 0;
    const int o1 = WS_WRDY + (tid < 224 ? tid : 0) * 16, o2 = WS_MODG + (4 * D + dd) * 8, o3 = WS_MODG + (3 * D + dd) * 8;
    const v2u g2 = ld8(rs, o2), g3 = ld8(rs, o3);
    const u32 vcb = finish_granule(rs, o2, tag, g2), vsb = finish_granule(rs, o3, tag, g3);
    if (tid < D) {
      ((LAS u16 *)(S + LD_CV))[tid] = (u16)(pkbf(1.0f + asf(vcb), 0.f) & 0xFFFFu);
      ((LAS u16 *)(S + LD_CV))[D + tid] = (u16)(vsb >> 16);
    }
    if (w < 4) {
#pragma unroll 1
      for (unsigned sp = 0; sp < SPIN_CAP; sp++) {
        const u32 wr = __builtin_amdgcn_raw_buffer_load_b32(rs, o1, 0, SC1);
        if (__all(wr == tag)) break;
        __builtin_amdgcn_s_sleep(4);
      }
      __builtin_amdgcn_fence(__ATOMIC_ACQUIRE, "wavefront");
    }
  }
  BAR();
  STAMP(5);
  {
    const int ph = 4 * sc + (w >> 1), s0 = 14 * (w & 1);
    v4u wf[14];
#pragma unroll
    for (int k = 0; k < 14; k++) wf[k] = ld16s(rs, lane16, WS_W1F + (ph * 28 + s0 + k) * 1024);
    PIN();
    f32x16 acc;
#pragma unroll
    for (int i = 0; i < 16; i++) acc[i] = 0.f;
    LAS unsigned char *cvp = S + LD_CV + ((lane & 31) == 1 ? D * 2 : 0) + (lane >> 5) * 16;
#pragma unroll
    for (int k = 0; k < 14; k++) acc = mfma32b(wf[k], *(LAS v4u *)(cvp + (s0 + k) * 32), acc);
    LAS float *pr = (LAS float *)(S) + w * 64;
    if ((lane & 31) < 2) {
#pragma unroll
      for (int i = 0; i < 16; i++) pr[(lane & 31) * 32 + (i & 3) + 8 * (i >> 2) + 4 * (lane >> 5)] = acc[i];
    }
  }
  BAR();
  if (sidx < 28 && tid < 256) {
    const int pl = tid >> 6, e = tid & 63;
    const float v = ((LAS float *)S)[(2 * pl) * 64 + e] + ((LAS float *)S)[(2 * pl + 1) * 64 + e];
    st8(rs, WS_CSB + ((4 * sidx + pl) * 64 + e) * 8, v2u{__builtin_bit_cast(u32, v), tag});
  }
  STAMP(6);
#pragma unroll 1
  for (int hf = 0; hf < 2; hf++) {
    if (w == 0) poll_counters(p, hf * 128 + g * 16, 16, 14u, lane);
    BAR();
    const int tau2 = 32 * hf + sidx;
    const int tp = lane & 15, rgp = (lane >> 4) + 4 * w;
    u32 xv[14];
#pragma unroll
    for (int i = 0; i < 14; i++)
      xv[i] = __builtin_amdgcn_raw_buffer_load_b32(rs, rgp * 128 + tp * 4, WS_X1T + ((tau2 >> 1) * D + 32 * i) * 128 + (tau2 & 1) * 64, SC1);
    PIN();
    f2 sm = {0.f, 0.f}, sq = {0.f, 0.f};
#pragma unroll
    for (int i = 0; i < 14; i++) {
      const f2 a = {bflo(xv[i]), bfhi(xv[i])};
      sm += a; sq = __builtin_elementwise_fma(a, a, sq);
    }
    LAS f32x4 *pr = (LAS f32x4 *)(S + 4096);
    pr[rgp * 16 + tp] = f32x4{sm.x, sm.y, sq.x, sq.y};
    BAR();
    if (tid < 128) {
      const int tok = tid >> 2, part = tid & 3;
      LAS float *pf = (LAS float *)pr + (part * 8 * 16 + (tok >> 1)) * 4 + (tok & 1);
      float s = 0.f, q = 0.f;
#pragma unroll
      for (int i = 0; i < 8; i++) { s += pf[i * 64]; q += pf[i * 64 + 2]; }
      s += dpp0<0xB1, 0xF>(s); q += dpp0<0xB1, 0xF>(q);
      s += dpp0<0x4E, 0xF>(s); q += dpp0<0x4E, 0xF>(q);
      const float mean = s * (1.0f / D);
      const float var = rbf(fmaxf(q * (1.0f / D) - mean * mean, 0.f));
      const float sd = rbf(__builtin_amdgcn_sqrtf(rbf(var + 1e-5f)));
      if (part == 0) st8(rs, WS_STAT2 + (32 * tau2 + tok) * 8, v2u{pkbf(mean, sd), tag});
    }
    STAMP(8 + hf);
  }
}

__global__ void k_mark2(unsigned long long *q) {
  const unsigned long long t_entry = __builtin_amdgcn_s_memrealtime();
  unsigned long long mx = 0;
  for (int i = threadIdx.x; i < 256; i += 64) { unsigned long long v = q[i]; mx = v > mx ? v : mx; }
  for (int o = 32; o; o >>= 1) { unsigned long long v = __shfl_xor(mx, o); mx = v > mx ? v : mx; }
  const unsigned long long t1 = __builtin_amdgcn_s_memrealtime(), d = (t_entry - mx) * MUL;
  while (__builtin_amdgcn_s_memrealtime() - t1 < d && d < 100000) __builtin_amdgcn_s_sleep(1);
}
__global__ void __launch_bounds__(NTHR, 2) k_main(Params p) {
  extern __shared__ __attribute__((aligned(16))) unsigned char smem[];
  LAS unsigned char *S = (LAS unsigned char *)smem;
  const int tid = threadIdx.x, lane = tid & 63, b = blockIdx.x;
  const int w = __builtin_amdgcn_readfirstlane(tid >> 6);
  const rsrc_t rs = __builtin_amdgcn_make_buffer_rsrc((void *)p.ws, 0, WS_END, 0x00020000);
  u32 pf;
  {
    unsigned long long pc;
    asm volatile("s_getpc_b64 %0" : "=s"(pc));
    pf = gld<u32>((const void *)(pc & ~127ull), (u32)(tid < PF_LINES ? tid : 0) * 128u);
  }
  const u32 epoch = __builtin_amdgcn_raw_buffer_load_b32(rs, WS_EPOCH, 0, SC1);
  STAMP(0);
  if ((b >> 3) < 28) main_role(p, S, rs, tid, lane, w, b, epoch);
  else side_role(p, S, rs, tid, lane, w, b, epoch);
  asm volatile("" ::"v"(pf));
  asm volatile("s_waitcnt vmcnt(0)" ::: "memory");
  __syncthreads();
  if (tid == 0) ((unsigned long long *)(p.ws + WS_DBG))[b] = __builtin_amdgcn_s_memrealtime();
}

}

extern "C" void kernel_launch(void *const *d_in, const int *in_sizes, int n_in, void *d_out, int out_size, void *d_ws,
                              size_t ws_size, hipStream_t stream) {
  using namespace mk;
  Params p;
  p.x = (const u16 *)d_in[0];
  p.Are = (const u16 *)d_in[1]; p.Aim = (const u16 *)d_in[2];
  p.Bre = (const u16 *)d_in[3]; p.Bim = (const u16 *)d_in[4];
  p.Cre = (const u16 *)d_in[5]; p.Cim = (const u16 *)d_in[6];
  p.logdt = (const u16 *)d_in[7]; p.Dsk = (const u16 *)d_in[8];
  p.wg = (const u16 *)d_in[10]; p.wu = (const u16 *)d_in[11]; p.wd = (const u16 *)d_in[12];
  p.cvec = (const u16 *)d_in[13]; p.wmod = (const u16 *)d_in[14];
  p.out = (u16 *)d_out;
  p.ws = (unsigned char *)d_ws;
  (void)hipFuncSetAttribute((const void *)k_main, hipFuncAttributeMaxDynamicSharedMemorySize, LDS_BYTES);
  k_main<<<NWG, NTHR, LDS_BYTES, stream>>>(p);
  k_mark2<<<1, 64, 0, stream>>>((unsigned long long *)(p.ws + WS_DBG));
}
```

```cpp
#include <hip/hip_runtime.h>
#include <stdint.h>

namespace mk {

typedef unsigned short u16;
typedef unsigned u32;
typedef u32 v4u __attribute__((ext_vector_type(4)));
typedef u32 v2u __attribute__((ext_vector_type(2)));
typedef float f2 __attribute__((ext_vector_type(2)));
typedef float f32x4 __attribute__((ext_vector_type(4)));
typedef float f32x16 __attribute__((ext_vector_type(16)));
typedef _Float16 h2 __attribute__((ext_vector_type(2)));
typedef _Float16 h8 __attribute__((ext_vector_type(8)));
typedef __bf16 b2 __attribute__((ext_vector_type(2)));
typedef __bf16 b8 __attribute__((ext_vector_type(8)));
typedef short s4 __attribute__((ext_vector_type(4)));
#define LAS __attribute__((address_space(3)))
#define GAS __attribute__((address_space(1)))
typedef GAS u32 gu32;
#define RLX_AGENT __ATOMIC_RELAXED, __HIP_MEMORY_SCOPE_AGENT

constexpr int L = 2048, D = 448, NS = 256, F = 1792;
constexpr int NWG = 256, NTHR = 512;
constexpr int SC1 = 16;
constexpr int PF_LINES = 190;

constexpr int WS_EPOCH = 0;
constexpr int WS_CNT = 4096;
constexpr int NCNT = 288;
constexpr int WS_STAT1 = WS_CNT + NCNT * 256;
constexpr int WS_MODG = WS_STAT1 + 2048 * 8;
constexpr int WS_CSB = WS_MODG + 2688 * 8;
constexpr int WS_STAT2 = WS_CSB + 112 * 64 * 8;
constexpr int WS_WRDY = WS_STAT2 + 2048 * 8;
constexpr int WS_XG = WS_WRDY + 256 * 16;
constexpr int WS_W1F = WS_XG + 128 * 224 * 128;
constexpr int WS_WDF = WS_W1F + 112 * 28 * 1024;
constexpr int WS_X1T = WS_WDF + 14 * 112 * 1024;
constexpr int WS_X1S = WS_X1T + 32 * 448 * 128;
constexpr int WS_HF = WS_X1S + 32 * 448 * 128;
constexpr int WS_DBG = WS_HF + 64 * 112 * 1024;
constexpr int WS_END = WS_DBG + (8 << 20);
static_assert(WS_STAT1 % 128 == 0 && WS_MODG % 128 == 0 && WS_CSB % 128 == 0 && WS_STAT2 % 128 == 0 && WS_XG % 128 == 0 && WS_W1F % 128 == 0, "align");

constexpr int PQ_ROW = 1056, PQ_CH = 57 * PQ_ROW;
constexpr int LD_PQ = 0;
constexpr int LD_KC = 0;
constexpr int KC_CP = 4160, KC_CH = 4 * KC_CP;
constexpr int LD_EX = 33280;
constexpr int LD_XB = 0;
constexpr int XB_SZ = 448 * 128;
constexpr int LD_RED = 0;
constexpr int RED_ROW = 272, RED_W = 32 * RED_ROW;
constexpr int LD_RES = 8 * RED_W;
constexpr int LD_KD = 120384;
constexpr int KD_CH = 784 * 2;
constexpr int LD_U = LD_KD + 2 * KD_CH;
constexpr int U_CH = 2304 * 2;
constexpr int LD_XS = LD_U + 2 * U_CH;
constexpr int LD_ST1 = 140928;
constexpr int LD_XROW = 120384;
constexpr int LD_WSTG = LD_XROW + 16128;
constexpr int WSTG_ROW = 464, WSTG_M = 16 * WSTG_ROW;
constexpr int LD_SMALL = LD_WSTG + 3 * WSTG_M;
constexpr int LD_STAT = LD_SMALL;
constexpr int LD_WMAX = LD_SMALL + 64;
constexpr int LD_CSL = LD_SMALL + 96;
constexpr int LD_STAMP = LD_CSL + 2048;
constexpr int LD_CV = LD_STAMP + 256;
constexpr int LD_END = LD_CV + 2 * 448 * 2;
constexpr int LDS_BYTES = 160 * 1024;
static_assert(LD_XS + 8192 <= LD_ST1 && LD_ST1 + 16384 <= LD_SMALL && LD_XS + 8192 <= LD_SMALL && LD_END <= LDS_BYTES && LD_CSL % 16 == 0 && LD_CV % 16 == 0, "lds");
static_assert(LD_RES + 4096 <= LD_KD, "lds red");

struct Params {
  const u16 *x, *Are, *Aim, *Bre, *Bim, *Cre, *Cim, *logdt, *Dsk, *wg, *wu, *wd, *cvec, *wmod;
  u16 *out;
  unsigned char *ws;
};

struct KTab { u16 v[256][16]; };
constexpr int bf16_round_int(int j) {
  if (j < 256) return j;
  int sh = 0; int t = j; while (t >= 256) { t >>= 1; sh++; }
  int q = j >> sh, rem = j & ((1 << sh) - 1), half = 1 << (sh - 1);
  if (rem > half || (rem == half && (q & 1))) q++;
  return q << sh;
}
constexpr int kd_index(int j) {
  if (j < 0) return 768;
  int l = bf16_round_int(j);
  if (l >= 2048) return 40 * 16;
  int k = l < 256 ? 0 : (l < 512 ? 1 : (l < 1024 ? 2 : 3));
  int base = k ? (128 << k) : 0;
  int rem = l - base;
  int a = rem >> 7, b = (rem >> 3) & 15, c = (rem & 7) >> k;
  int row = (k ? 8 + 8 * k : 0) + a * (8 >> k) + c;
  return row * 16 + b;
}
constexpr KTab make_ktab() {
  KTab t{};
  for (int i = 0; i < 256; i++)
    for (int j = 0; j < 16; j++) t.v[i][j] = (u16)(2 * (j < 11 ? kd_index(8 * i - 3 + j) : 768));
  return t;
}
__device__ const KTab g_ktab = make_ktab();

__device__ __forceinline__ float asf(u32 v) { return __builtin_bit_cast(float, v); }
__device__ __forceinline__ h2 ash2(u32 v) { return __builtin_bit_cast(h2, v); }
__device__ __forceinline__ float bf2f(u32 v) { return __builtin_bit_cast(float, v << 16); }
__device__ __forceinline__ float bflo(u32 v) { return __builtin_bit_cast(float, v << 16); }
__device__ __forceinline__ float bfhi(u32 v) { return __builtin_bit_cast(float, v & 0xFFFF0000u); }
__device__ __forceinline__ u32 pkbf(float a, float b) {
  return __builtin_bit_cast(u32, __builtin_convertvector(f2{a, b}, b2));
}
__device__ __forceinline__ u32 pkh(float a, float b) {
  return __builtin_bit_cast(u32, __builtin_convertvector(f2{a, b}, h2));
}
__device__ __forceinline__ float rbf(float a) { return bflo(pkbf(a, a)); }
__device__ __forceinline__ f2 cmul(f2 a, f2 b) {
  f2 r;
  asm("v_pk_mul_f32 %0, %1, %2 op_sel_hi:[0,1]\n\t"
      "v_pk_fma_f32 %0, %1, %2, %0 op_sel:[1,1,0] op_sel_hi:[1,0,1] neg_lo:[1,0,0]"
      : "=&v"(r) : "v"(a), "v"(b));
  return r;
}
__device__ __forceinline__ float dot2bf(u32 a, u32 b, float c) {
  return __builtin_amdgcn_fdot2_f32_bf16(__builtin_bit_cast(b2, a), __builtin_bit_cast(b2, b), c, false);
}
template <class T> __device__ __forceinline__ T gld(const void *base, u32 off) { return *(const GAS T *)((const GAS char *)base + off); }
template <int CTRL, int RM> __device__ __forceinline__ float dpp0(float v) {
  return __builtin_bit_cast(float, __builtin_amdgcn_update_dpp(0, __builtin_bit_cast(int, v), CTRL, RM, 0xF, true));
}
__device__ __forceinline__ float wave_sum(float v) {
  v += dpp0<0x111, 0xF>(v);
  v += dpp0<0x112, 0xF>(v);
  v += dpp0<0x114, 0xF>(v);
  v += dpp0<0x118, 0xF>(v);
  v += dpp0<0x142, 0xA>(v);
  v += dpp0<0x143, 0xC>(v);
  return __builtin_bit_cast(float, __builtin_amdgcn_readlane(__builtin_bit_cast(int, v), 63));
}
__device__ __forceinline__ float wave_max_nonneg(float v) {
  v = fmaxf(v, dpp0<0x111, 0xF>(v));
  v = fmaxf(v, dpp0<0x112, 0xF>(v));
  v = fmaxf(v, dpp0<0x114, 0xF>(v));
  v = fmaxf(v, dpp0<0x118, 0xF>(v));
  v = fmaxf(v, dpp0<0x142, 0xA>(v));
  v = fmaxf(v, dpp0<0x143, 0xC>(v));
  return __builtin_bit_cast(float, __builtin_amdgcn_readlane(__builtin_bit_cast(int, v), 63));
}
__device__ __forceinline__ v2u trread(LAS unsigned char *p) {
  return __builtin_bit_cast(v2u, __builtin_amdgcn_ds_read_tr16_b64_v4i16((LAS s4 *)p));
}
__device__ __forceinline__ v4u tr8(LAS unsigned char *p0, LAS unsigned char *p1) {
  v2u a = trread(p0), b = trread(p1);
  return v4u{a.x, a.y, b.x, b.y};
}
__device__ __forceinline__ f32x4 mfma16h(v4u a, v4u b, f32x4 c) {
  return __builtin_amdgcn_mfma_f32_16x16x32_f16(__builtin_bit_cast(h8, a), __builtin_bit_cast(h8, b), c, 0, 0, 0);
}
__device__ __forceinline__ f32x16 mfma32b(v4u a, v4u b, f32x16 c) {
  return __builtin_amdgcn_mfma_f32_32x32x16_bf16(__builtin_bit_cast(b8, a), __builtin_bit_cast(b8, b), c, 0, 0, 0);
}
#define BAR() asm volatile("s_waitcnt lgkmcnt(0)\n\ts_barrier" ::: "memory")
#define DRAIN() asm volatile("s_waitcnt vmcnt(0)" ::: "memory")
#define PIN() do { asm volatile("" ::: "memory"); __builtin_amdgcn_sched_barrier(0); } while (0)
typedef __amdgpu_buffer_rsrc_t rsrc_t;
__device__ __forceinline__ v4u ld16(rsrc_t rs, int off) { return __builtin_amdgcn_raw_buffer_load_b128(rs, off, 0, SC1); }
__device__ __forceinline__ v4u ld16s(rsrc_t rs, int voff, int soff) { return __builtin_amdgcn_raw_buffer_load_b128(rs, voff, soff, SC1); }
__device__ __forceinline__ void st16s(rsrc_t rs, int voff, int soff, v4u v) { __builtin_amdgcn_raw_buffer_store_b128(v, rs, voff + soff, 0, SC1); }
__device__ __forceinline__ v2u ld8(rsrc_t rs, int off) { return __builtin_amdgcn_raw_buffer_load_b64(rs, off, 0, SC1); }
__device__ __forceinline__ void st16(rsrc_t rs, int off, v4u v) { __builtin_amdgcn_raw_buffer_store_b128(v, rs, off, 0, SC1); }
__device__ __forceinline__ void st8(rsrc_t rs, int off, v2u v) { __builtin_amdgcn_raw_buffer_store_b64(v, rs, off, 0, SC1); }
__device__ __forceinline__ void st4(rsrc_t rs, int off, u32 v) { __builtin_amdgcn_raw_buffer_store_b32(v, rs, off, 0, SC1); }

constexpr unsigned SPIN_CAP = 1u << 14;

#define STAMP(i)

__device__ __forceinline__ u32 peek_counters(const Params &p, int idx0, int ns, int lane) {
  return __hip_atomic_load((gu32 *)(p.ws + WS_CNT) + (idx0 + (lane < ns ? lane : 0)) * 64, RLX_AGENT);
}
__device__ __forceinline__ void finish_counters(const Params &p, int idx0, int ns, u32 want, int lane, u32 v) {
#pragma unroll 1
  for (unsigned sp = 0; sp < SPIN_CAP; sp++) {
    if (__all(v >= want)) break;
    __builtin_amdgcn_s_sleep(1);
    v = peek_counters(p, idx0, ns, lane);
  }
  __builtin_amdgcn_fence(__ATOMIC_ACQUIRE, "wavefront");
}
__device__ __forceinline__ void poll_counters(const Params &p, int idx0, int ns, u32 want, int lane) {
  finish_counters(p, idx0, ns, want, lane, peek_counters(p, idx0, ns, lane));
}
__device__ __forceinline__ u32 finish_granule(rsrc_t rs, int off, u32 tag, v2u g) {
#pragma unroll 1
  for (unsigned sp = 0; sp < SPIN_CAP; sp++) {
    if (__all(g.y == tag)) break;
    __builtin_amdgcn_s_sleep(1);
    g = ld8(rs, off);
  }
  __builtin_amdgcn_fence(__ATOMIC_ACQUIRE, "wavefront");
  return g.x;
}

template <int ROW> __device__ __forceinline__ void pqw4(u32 m0v, f2 a, f2 b, f2 c, f2 d) {
  const u32 h0 = pkh(a.x, a.y), h1 = pkh(b.x, b.y), h2_ = pkh(c.x, c.y), h3 = pkh(d.x, d.y);
  asm volatile("s_mov_b32 m0, %4\n\ts_nop 0\n\tds_write_addtid_b32 %0 offset:%c5\n\tds_write_addtid_b32 %1 offset:%c6\n\t"
               "ds_write_addtid_b32 %2 offset:%c7\n\tds_write_addtid_b32 %3 offset:%c8"
               ::"v"(h0), "v"(h1), "v"(h2_), "v"(h3), "s"(m0v), "i"(ROW * PQ_ROW), "i"((ROW + 1) * PQ_ROW),
               "i"((ROW + 2) * PQ_ROW), "i"((ROW + 3) * PQ_ROW) : "memory");
}
template <int ROW> __device__ __forceinline__ void pqw1(u32 m0v, f2 a) {
  const u32 h0 = pkh(a.x, a.y);
  asm volatile("s_mov_b32 m0, %1\n\ts_nop 0\n\tds_write_addtid_b32 %0 offset:%c2" ::"v"(h0), "s"(m0v), "i"(ROW * PQ_ROW) : "memory");
}
__device__ __forceinline__ f2 zpow(float al, float rv) {
  const float e = __builtin_amdgcn_exp2f(al);
  const float r = __builtin_amdgcn_fractf(rv);
  return f2{e * __builtin_amdgcn_cosf(r), e * __builtin_amdgcn_sinf(r)};
}
__device__ __forceinline__ float pow2b(int e) { return asf((u32)e << 23); }

__device__ __forceinline__ void ln1_rows(LAS unsigned char *S, v4u x0, v4u x1, int w, int lane) {
  constexpr u32 ONES = 0x3F803F80u;
  float v[4];
  v[0] = dot2bf(x0.w, ONES, dot2bf(x0.z, ONES, dot2bf(x0.y, ONES, dot2bf(x0.x, ONES, 0.f))));
  v[1] = dot2bf(x0.w, x0.w, dot2bf(x0.z, x0.z, dot2bf(x0.y, x0.y, dot2bf(x0.x, x0.x, 0.f))));
  v[2] = dot2bf(x1.w, ONES, dot2bf(x1.z, ONES, dot2bf(x1.y, ONES, dot2bf(x1.x, ONES, 0.f))));
  v[3] = dot2bf(x1.w, x1.w, dot2bf(x1.z, x1.z, dot2bf(x1.y, x1.y, dot2bf(x1.x, x1.x, 0.f))));
  if (lane < 56) {
    *(LAS v4u *)(S + LD_XROW + (2 * w) * 960 + lane * 16) = x0;
    *(LAS v4u *)(S + LD_XROW + (2 * w + 1) * 960 + lane * 16) = x1;
  }
#pragma unroll
  for (int i = 0; i < 4; i++) v[i] = lane < 56 ? v[i] : 0.f;
#pragma unroll
  for (int i = 0; i < 4; i++) v[i] += dpp0<0x111, 0xF>(v[i]);
#pragma unroll
  for (int i = 0; i < 4; i++) v[i] += dpp0<0x112, 0xF>(v[i]);
#pragma unroll
  for (int i = 0; i < 4; i++) v[i] += dpp0<0x114, 0xF>(v[i]);
#pragma unroll
  for (int i = 0; i < 4; i++) v[i] += dpp0<0x118, 0xF>(v[i]);
#pragma unroll
  for (int i = 0; i < 4; i++) v[i] += dpp0<0x142, 0xA>(v[i]);
#pragma unroll
  for (int i = 0; i < 4; i++) v[i] += dpp0<0x143, 0xC>(v[i]);
  const float s1 = asf(__builtin_amdgcn_readlane(__builtin_bit_cast(int, v[2]), 63));
  const float q1 = asf(__builtin_amdgcn_readlane(__builtin_bit_cast(int, v[3]), 63));
  const float s = lane == 62 ? s1 : v[0], q = lane == 62 ? q1 : v[1];
  const float mean = s * (1.0f / D);
  const float var = rbf(fmaxf(q * (1.0f / D) - mean * mean, 0.f));
  const float sd = rbf(__builtin_amdgcn_sqrtf(rbf(var + 1e-5f)));
  if (lane >= 62) *(LAS u32 *)(S + LD_STAT + (2 * w + 63 - lane) * 4) = pkbf(mean, sd);
}
__device__ __forceinline__ void publish_xt(LAS unsigned char *S, const rsrc_t rs, int lane, int w, int tau, u32 tag) {
  const int G = lane >> 4, e = (lane >> 2) & 3, pp = lane & 3;
  LAS unsigned char *ax = S + LD_XROW + (4 * G + e) * 960 + 4 * pp * 2 + w * 32;
  const int go = WS_XG + tau * (224 * 128) + w * 1024 + (lane & 15) * 64 + G * 16;
  const v2u v0 = trread(ax), v1 = trread(ax + 256), v2 = trread(ax + 512), v3 = trread(ax + (w < 4 ? 768 : 512));
  st16(rs, go, v4u{v0.x, tag, v0.y, tag});
  st16(rs, go + 8192, v4u{v1.x, tag, v1.y, tag});
  st16(rs, go + 16384, v4u{v2.x, tag, v2.y, tag});
  if (w < 4) st16(rs, go + 24576, v4u{v3.x, tag, v3.y, tag});
  if (w == 7 && lane < 16) st8(rs, WS_STAT1 + (16 * tau + lane) * 8, v2u{*(LAS u32 *)(S + LD_STAT + lane * 4), tag});
}

__device__ __forceinline__ void main_role(const Params &p, LAS unsigned char *S, const rsrc_t rs, const int tid,
                                          const int lane, const int w, const int b, const u32 epoch) {
  const int g = b & 7, r = b >> 3, m = 28 * g + r;
  const int cch = w >> 2, t8 = tid & 255, dch = 2 * m + cch;
  const int lane16 = lane * 16;
  const bool xprod = (r & 1) == 0;
  const int tau = 16 * g + (r >> 1), mi = 14 * g + (r >> 1);
  const u32 tm = tid < 504 ? tid : 503, mrow = (tm * 21846u) >> 16, mpc = tm - 3 * mrow;
  v4u rl[3];
  u32 cv[3];
  {
    const void *rb = xprod ? (const void *)p.x : (const void *)p.wmod;
    const u32 lo = xprod ? (u32)(lane < 56 ? lane : 55) * 16u : mrow * (12u * D) + mpc * 16u;
    const u32 u0 = xprod ? (u32)(16 * tau + 2 * w) * (2u * D) : (u32)mi * 48u;
    const u32 st = xprod ? 2u * D : 168u * 12u * D;
    const bool v2 = xprod || mrow < 112;
    rl[0] = gld<v4u>(rb, lo + u0);
    rl[1] = gld<v4u>(rb, lo + u0 + st);
    rl[2] = gld<v4u>(rb, lo + u0 + (xprod ? st : (v2 ? 2 * st : 0u)));
    cv[0] = gld<u16>(p.cvec, mrow * 2u);
    cv[1] = gld<u16>(p.cvec, mrow * 2u + 336u);
    cv[2] = gld<u16>(p.cvec, (mrow < 112 ? mrow : 111u) * 2u + 672u);
  }
  PIN();
  const u32 po = (u32)(dch * NS + t8) * 2u;
  const u32 pAre = gld<u16>(p.Are, po), pAim = gld<u16>(p.Aim, po), pBre = gld<u16>(p.Bre, po), pBim = gld<u16>(p.Bim, po);
  const u32 pCre = gld<u16>(p.Cre, po), pCim = gld<u16>(p.Cim, po);
  const u32 sLdt = *(const __attribute__((address_space(4))) u32 *)(p.logdt + 2 * m);
  const u32 sDsk = *(const __attribute__((address_space(4))) u32 *)(p.Dsk + 2 * m);
  const u32 pLdt = cch ? sLdt >> 16 : sLdt & 0xFFFFu, pDsk = cch ? sDsk >> 16 : sDsk & 0xFFFFu;
  const u32 tc = tid < 448 ? tid : 447, srow = (tc * 2341u) >> 16, scol = tc - 28 * srow;
  const v4u ws0 = gld<v4u>(p.wg, srow * (2u * F) + scol * 16u + (u32)(16 * (m >> 3) * F + 224 * (m & 7)) * 2u);
  const v4u ws1 = gld<v4u>(p.wu, srow * (2u * F) + scol * 16u + (u32)(16 * (m >> 3) * F + 224 * (m & 7)) * 2u);
  const v4u ws2 = gld<v4u>(p.wd, srow * (2u * D) + scol * 16u + (u32)(16 * (m >> 1) * D + 224 * (m & 1)) * 2u);
  PIN();
  asm volatile("" : "+v"(rl[0]), "+v"(rl[1]), "+v"(rl[2]));
  const u32 tag = __builtin_amdgcn_readfirstlane(epoch) + 1u;

  if (xprod) {
    ln1_rows(S, rl[0], rl[1], w, lane);
  } else {
    f2 a[4] = {f2{0.f, 0.f}, f2{0.f, 0.f}, f2{0.f, 0.f}, f2{0.f, 0.f}};
#pragma unroll
    for (int i = 0; i < 3; i++) {
      const float cx = bf2f(cv[i]);
      float sl = rbf(cx * __builtin_amdgcn_rcpf(1.0f + __builtin_amdgcn_exp2f(cx * -1.44269504088896f)));
      if (i == 2) sl = mrow < 112 ? sl : 0.f;
      const f2 s2 = {sl, sl};
      a[0] = __builtin_elementwise_fma(s2, f2{bflo(rl[i].x), bfhi(rl[i].x)}, a[0]);
      a[1] = __builtin_elementwise_fma(s2, f2{bflo(rl[i].y), bfhi(rl[i].y)}, a[1]);
      a[2] = __builtin_elementwise_fma(s2, f2{bflo(rl[i].z), bfhi(rl[i].z)}, a[2]);
      a[3] = __builtin_elementwise_fma(s2, f2{bflo(rl[i].w), bfhi(rl[i].w)}, a[3]);
    }
    if (tid < 504) {
      LAS f32x4 *mp = (LAS f32x4 *)(S + LD_XROW + tm * 32);
      mp[0] = f32x4{a[0].x, a[0].y, a[1].x, a[1].y}; mp[1] = f32x4{a[2].x, a[2].y, a[3].x, a[3].y};
    }
  }
  if (tid < 448) {
    LAS unsigned char *wp = S + LD_WSTG + srow * WSTG_ROW + scol * 16;
    *(LAS v4u *)(wp) = ws0;
    *(LAS v4u *)(wp + WSTG_M) = ws1;
    *(LAS v4u *)(wp + 2 * WSTG_M) = ws2;
  }
  asm volatile("" ::"v"(ws0), "v"(ws1), "v"(ws2), "v"(pAre), "v"(pAim), "v"(pBre), "v"(pBim), "v"(pCre), "v"(pCim));
  STAMP(1);
  BAR();
  if (xprod) {
    publish_xt(S, rs, lane, w, tau, tag);
  } else if (w < 6) {
    const int col = tid >> 4, part = tid & 15;
    LAS float *mp = (LAS float *)(S + LD_XROW) + part * 24 + col;
    float sm = part < 8 ? mp[160 * 24] : 0.f;
#pragma unroll
    for (int i = 0; i < 10; i++) sm += mp[i * 16 * 24];
    sm += dpp0<0x111, 0xF>(sm); sm += dpp0<0x112, 0xF>(sm); sm += dpp0<0x114, 0xF>(sm); sm += dpp0<0x118, 0xF>(sm);
    if (part == 15) st8(rs, WS_MODG + (24 * mi + col) * 8, v2u{__builtin_bit_cast(u32, rbf(sm)), tag});
  }
  PIN();
  STAMP(2);

  const float dt = rbf(__builtin_amdgcn_exp2f(bf2f(pLdt) * 1.44269504088896f));
  const float al = (dt * bf2f(pAre)) * 1.44269504088896f;
  const float rv = (dt * bf2f(pAim)) * 0.159154943091895f;
  const f2 Wn = cmul(f2{bf2f(pCre), -bf2f(pCim)}, f2{dt * bf2f(pBre), -dt * bf2f(pBim)});
  {
    const float mx = wave_max_nonneg(fmaxf(fabsf(Wn.x), fabsf(Wn.y)));
    if (lane == 0) *(LAS float *)(S + LD_WMAX + w * 4) = mx;
  }
  const u32 m0v = (u32)(LD_PQ + (w >> 2) * PQ_CH + (w & 3) * 256);
  const f2 p1 = zpow(al, rv), z8 = zpow(al * 8.f, rv * -8.f), Z1 = zpow(al * 128.f, rv * 128.f);
  const f2 p2 = cmul(p1, p1), p3 = cmul(p2, p1), p4 = cmul(p2, p2), p5 = cmul(p4, p1), p6 = cmul(p4, p2), p7 = cmul(p4, p3);
  pqw4<0>(m0v, f2{1.f, 0.f}, p1, p2, p3);
  pqw4<4>(m0v, p4, p5, p6, p7);
  pqw4<8>(m0v, Z1, cmul(Z1, p1), cmul(Z1, p2), cmul(Z1, p3));
  pqw4<12>(m0v, cmul(Z1, p4), cmul(Z1, p5), cmul(Z1, p6), cmul(Z1, p7));
  PIN();
  v4u wdfrag;
  {
    const int G = lane >> 4, e = (lane >> 2) & 3, pp = lane & 3, h = G >> 1;
    LAS unsigned char *a0 = S + LD_WSTG + (G & 1) * WSTG_M + (8 * h + e) * WSTG_ROW + (16 * w + 4 * pp) * 2;
    LAS unsigned char *a1 = a0 + (w < 6 ? 256 : 0);
    LAS unsigned char *a2 = S + LD_WSTG + 2 * WSTG_M + (4 * h + e) * WSTG_ROW + (32 * (w < 7 ? w : 6) + 16 * (G & 1) + 4 * pp) * 2;
    const v4u v0 = tr8(a0, a0 + 4 * WSTG_ROW), v1 = tr8(a1, a1 + 4 * WSTG_ROW);
    wdfrag = tr8(a2, a2 + 8 * WSTG_ROW);
    const int o1 = WS_W1F + ((14 * (m & 7) + w) * 28 + (m >> 3)) * 1024 + lane16;
    st16(rs, o1, v0);
    if (w < 6) st16(rs, o1 + 8 * 28 * 1024, v1);
    if (w == 7 && lane < 2 && m + 224 * lane < NCNT) st4(rs, WS_CNT + (m + 224 * lane) * 256, 0u);
  }
  PIN();
  STAMP(3);
  v4u xg[2], sg[2], mg[4], kt0;
  v2u kt1;
  int ebW;
  const int hch = (tid >> 2) & 1, l4 = 16 * (tid >> 3) + 4 * (tid & 3);
  const int xo = ((tid >> 3) * 224) * 128 + (tid & 7) * 16;
  const int xu = WS_XG + m * 128, mo = WS_MODG + 2 * m * 8;
#define LD_HANDOFF_A() do { \
    sg[0] = ld16s(rs, tid * 16, WS_STAT1); sg[1] = ld16s(rs, tid * 16, WS_STAT1 + 8192); \
    xg[0] = ld16s(rs, xo, xu); xg[1] = ld16s(rs, xo, xu + 64 * 224 * 128); \
    mg[0] = ld16s(rs, 0, mo + 0 * D * 8); mg[1] = ld16s(rs, 0, mo + 1 * D * 8); \
    mg[2] = ld16s(rs, 0, mo + 2 * D * 8); mg[3] = ld16s(rs, 0, mo + 4 * D * 8); } while (0)
  {
    const f2 Z2 = cmul(Z1, Z1), Z3 = cmul(Z2, Z1), Z4 = cmul(Z2, Z2);
    pqw4<16>(m0v, Z2, cmul(Z2, p2), cmul(Z2, p4), cmul(Z2, p6));
    pqw4<20>(m0v, Z3, cmul(Z3, p2), cmul(Z3, p4), cmul(Z3, p6));
    const f2 Z5 = cmul(Z4, Z1), Z6 = cmul(Z4, Z2), Z7 = cmul(Z4, Z3), Z8 = cmul(Z4, Z4);
    pqw4<24>(m0v, Z4, cmul(Z4, p4), Z5, cmul(Z5, p4));
    pqw4<28>(m0v, Z6, cmul(Z6, p4), Z7, cmul(Z7, p4));
    pqw4<32>(m0v, Z8, cmul(Z8, Z1), cmul(Z8, Z2), cmul(Z8, Z3));
    pqw4<36>(m0v, cmul(Z8, Z4), cmul(Z8, Z5), cmul(Z8, Z6), cmul(Z8, Z7));
    pqw1<40>(m0v, cmul(Z8, Z8));
    STAMP(20);
    kt0 = gld<v4u>(&g_ktab, (u32)t8 * 32u);
    kt1 = gld<v2u>(&g_ktab, (u32)t8 * 32u + 16u);
    LD_HANDOFF_A();
    PIN();
    asm volatile("s_waitcnt vmcnt(10) ; PUBWAIT" ::: "memory");
    BAR();
    if (tid == 0) st4(rs, WS_WRDY + m * 16, tag);
    STAMP(21);
    {
      const f32x4 mx4 = *(LAS f32x4 *)(S + LD_WMAX + (w & 4) * 4);
      const float mx = fmaxf(fmaxf(mx4[0], mx4[1]), fmaxf(mx4[2], mx4[3]));
      ebW = (int)((__builtin_bit_cast(u32, mx) >> 23) & 0xFF);
      ebW = ebW < 14 ? 14 : (ebW > 253 ? 253 : ebW);
    }
    const f2 z16 = cmul(z8, z8), z24 = cmul(z16, z8), z32 = cmul(z16, z16);
    f2 q0 = Wn * pow2b(267 - ebW), q1 = cmul(q0, z8), q2 = cmul(q0, z16), q3 = cmul(q0, z24);
    pqw4<41>(m0v, q0, q1, q2, q3);
    q0 = cmul(q0, z32); q1 = cmul(q1, z32); q2 = cmul(q2, z32); q3 = cmul(q3, z32);
    pqw4<45>(m0v, q0, q1, q2, q3);
    q0 = cmul(q0, z32); q1 = cmul(q1, z32); q2 = cmul(q2, z32); q3 = cmul(q3, z32);
    pqw4<49>(m0v, q0, q1, q2, q3);
    q0 = cmul(q0, z32); q1 = cmul(q1, z32); q2 = cmul(q2, z32); q3 = cmul(q3, z32);
    pqw4<53>(m0v, q0, q1, q2, q3);
  }
  STAMP(4);
  BAR();
  {
    const int tile = w & 3;
    if (tile < 3) {
      LAS unsigned char *pa = S + LD_PQ + cch * PQ_CH + (16 * tile + (lane & 15)) * PQ_ROW + (lane >> 4) * 16;
      LAS unsigned char *pb = S + LD_PQ + cch * PQ_CH + (41 + (lane & 15)) * PQ_ROW + (lane >> 4) * 16;
      f32x4 acc0 = {0.f, 0.f, 0.f, 0.f}, acc1 = acc0;
      v4u fa[16], fb[16];
#pragma unroll
      for (int ks = 0; ks < 16; ks++) { fa[ks] = *(LAS v4u *)(pa + ks * 64); fb[ks] = *(LAS v4u *)(pb + ks * 64); }
      PIN();
#pragma unroll
      for (int ks = 0; ks < 16; ks += 2) { acc0 = mfma16h(fa[ks], fb[ks], acc0); acc1 = mfma16h(fa[ks + 1], fb[ks + 1], acc1); }
      acc0 = (acc0 + acc1) * (1.0f / 256.0f);
      LAS u16 *kd = (LAS u16 *)(S + LD_KD + cch * KD_CH) + (16 * tile + 4 * (lane >> 4)) * 16 + (lane & 15);
      const u32 k01 = pkh(acc0[0], acc0[1]), k23 = pkh(acc0[2], acc0[3]);
      kd[0] = (u16)k01; kd[16] = (u16)(k01 >> 16); kd[32] = (u16)k23; kd[48] = (u16)(k23 >> 16);
    } else if (lane < 16) {
      ((LAS u16 *)(S + LD_KD + cch * KD_CH))[768 + lane] = 0;
    }
  }
#pragma unroll 1
  for (unsigned sp = 0; sp < SPIN_CAP; sp++) {
    const u32 d = ((xg[0].y ^ tag) | (xg[0].w ^ tag) | (xg[1].y ^ tag) | (xg[1].w ^ tag)) |
                  ((sg[0].y ^ tag) | (sg[0].w ^ tag) | (sg[1].y ^ tag) | (sg[1].w ^ tag)) |
                  ((mg[0].y ^ tag) | (mg[0].w ^ tag) | (mg[1].y ^ tag) | (mg[1].w ^ tag)) |
                  ((mg[2].y ^ tag) | (mg[2].w ^ tag) | (mg[3].y ^ tag) | (mg[3].w ^ tag));
    if (__all(d == 0)) break;
    __builtin_amdgcn_s_sleep(1);
    LD_HANDOFF_A();
  }
#undef LD_HANDOFF_A
  __builtin_amdgcn_fence(__ATOMIC_ACQUIRE, "wavefront");
  *(LAS f32x4 *)(S + LD_ST1 + tid * 16) = f32x4{bflo(sg[0].x), __builtin_amdgcn_rcpf(bfhi(sg[0].x)), bflo(sg[0].z), __builtin_amdgcn_rcpf(bfhi(sg[0].z))};
  *(LAS f32x4 *)(S + LD_ST1 + 8192 + tid * 16) = f32x4{bflo(sg[1].x), __builtin_amdgcn_rcpf(bfhi(sg[1].x)), bflo(sg[1].z), __builtin_amdgcn_rcpf(bfhi(sg[1].z))};
  STAMP(5);
  BAR();
  const int fo = WS_WRDY + t8 * 16;
  u32 wr = __builtin_amdgcn_raw_buffer_load_b32(rs, fo, 0, SC1);
  PIN();

  {
    LAS unsigned char *kd = S + LD_KD + cch * KD_CH;
    u32 v[11];
    const u32 ix[6] = {kt0.x, kt0.y, kt0.z, kt0.w, kt1.x, kt1.y};
#pragma unroll
    for (int i = 0; i < 11; i++) v[i] = *(LAS u16 *)(kd + ((i & 1) ? (ix[i >> 1] >> 16) : (ix[i >> 1] & 0xFFFFu)));
    LAS unsigned char *kc = S + LD_KC + cch * KC_CH + (16 + 8 * t8) * 2;
    u32 pr[10];
#pragma unroll
    for (int i = 0; i < 10; i++) pr[i] = v[i] | (v[i + 1] << 16);
#pragma unroll
    for (int e = 0; e < 4; e++) *(LAS v4u *)(kc + e * KC_CP) = v4u{pr[3 - e], pr[5 - e], pr[7 - e], pr[9 - e]};
    if (t8 < 8) *(LAS v4u *)(S + LD_KC + cch * KC_CH + (t8 >> 1) * KC_CP + (t8 & 1) * 16) = v4u{0, 0, 0, 0};
  }
  float A1, A2, c2;
  {
    const float sah = asf(hch ? mg[0].z : mg[0].x), c1h = rbf(1.0f + asf(hch ? mg[1].z : mg[1].x));
    int eh = (int)((__builtin_bit_cast(u32, 21.5f * fabsf(c1h) + fabsf(sah)) >> 23) & 0xFF);
    eh = eh < 14 ? 14 : (eh > 253 ? 253 : eh);
    const float sH = pow2b(267 - eh);
    const f2 c1s = {c1h * sH, c1h * sH}, sas = {sah * sH, sah * sH};
#pragma unroll
    for (int i = 0; i < 2; i++) {
      const u32 x01 = xg[i].x, x23 = xg[i].z;
      const f32x4 sa_ = *(LAS f32x4 *)(S + LD_ST1 + (l4 + 1024 * i) * 8), sb_ = *(LAS f32x4 *)(S + LD_ST1 + (l4 + 1024 * i) * 8 + 16);
      const f2 h01 = __builtin_elementwise_fma((f2{bflo(x01), bfhi(x01)} - f2{sa_[0], sa_[2]}) * f2{sa_[1], sa_[3]}, c1s, sas);
      const f2 h23 = __builtin_elementwise_fma((f2{bflo(x23), bfhi(x23)} - f2{sb_[0], sb_[2]}) * f2{sb_[1], sb_[3]}, c1s, sas);
      *(LAS v2u *)(S + LD_U + hch * U_CH + (256 + l4 + 1024 * i) * 2) = v2u{pkh(h01.x, h01.y), pkh(h23.x, h23.y)};
      *(LAS v2u *)(S + LD_XS + hch * 4096 + (l4 + 1024 * i) * 2) = v2u{x01, x23};
    }
    if (t8 < 32) *(LAS v4u *)(S + LD_U + cch * U_CH + 16 * t8) = v4u{0, 0, 0, 0};
    const float sac = asf(cch ? mg[0].z : mg[0].x), c1c = rbf(1.0f + asf(cch ? mg[1].z : mg[1].x));
    int ec = (int)((__builtin_bit_cast(u32, 21.5f * fabsf(c1c) + fabsf(sac)) >> 23) & 0xFF);
    ec = ec < 14 ? 14 : (ec > 253 ? 253 : ec);
    const float ga = asf(cch ? mg[2].z : mg[2].x);
    const float iH = pow2b(ec - 13);
    A1 = ga * iH * pow2b(ebW - 5);
    A2 = ga * bf2f(pDsk) * iH;
    c2 = rbf(1.0f + asf(cch ? mg[3].z : mg[3].x));
  }
#pragma unroll 1
  for (unsigned sp = 0; sp < SPIN_CAP; sp++) {
    if (__all(wr == tag)) break;
    __builtin_amdgcn_s_sleep(1);
    wr = __builtin_amdgcn_raw_buffer_load_b32(rs, fo, 0, SC1);
  }
  __builtin_amdgcn_fence(__ATOMIC_ACQUIRE, "wavefront");
  STAMP(6);
  BAR();
  if (b == 0 && tid == 64) st4(rs, WS_EPOCH, tag);
  if (w < 7) st16(rs, WS_WDF + ((7 * (m & 1) + w) * 112 + (m >> 1)) * 1024 + lane16, wdfrag);
  PIN();

  const int fc = 7 * (g & 1) + r % 7, tt = 4 * (g >> 1) + r / 7;
  const int phi = 8 * fc + w;
  const int dtile = r % 7, t2 = 4 * g + r / 7;
  v4u w1[28];
  const int w1o = WS_W1F + phi * 28 * 1024;
#define LDW1(s) do { w1[s] = ld16s(rs, lane16, w1o + (s) * 1024); PIN(); } while (0)
  v2u csg_;
  const int cso = WS_CSB + (phi * 64 + lane) * 8;

  {
    const int c = cch, pq = w & 3;
    const int a15 = lane & 15, q = lane >> 4, e = (lane >> 2) & 3, pp = lane & 3;
    LAS unsigned char *ka = S + LD_KC + c * KC_CH + e * KC_CP + (16 + 16 * (q >> 1) - 8 * (q & 1) + 4 * pp) * 2 + pq * 128 - 8;
    LAS unsigned char *ub = S + LD_U + c * U_CH + 512 + 32 * a15 - 32 * (q >> 1) + 16 * (q & 1) - pq * 128 - 64;
    const int l16 = 16 * a15 + 4 * q;
    const int xoff = ((q & 1) ? WS_X1S : WS_X1T) + ((a15 >> 2) * D + dch) * 128 + (16 * (a15 & 3) + 8 * (q >> 1)) * 2;
#define RD_A(i) tr8(ka + (i) * 512 + pi * 64 + 8, ka + (i) * 512 + pi * 64)
#define RD_B(i) (*(LAS v4u *)(ub + (i) * 512 + 64 - pi * 64))
#pragma unroll
    for (int hf = 0; hf < 2; hf++) {
      LAS unsigned char *ex = S + LD_EX + hf * 32768 + c * 16384 + lane16;
      f32x4 acc[4];
#pragma unroll
      for (int i = 0; i < 4; i++) acc[i] = f32x4{0.f, 0.f, 0.f, 0.f};
#pragma unroll
      for (int pi = 0; pi < 2; pi++) {
        v4u A[8], B[8];
        if (hf == 0) {
          A[0] = RD_A(0); B[0] = RD_B(0); B[1] = RD_B(1); B[2] = RD_B(2); B[3] = RD_B(3);
          A[1] = RD_A(1); A[2] = RD_A(2); A[3] = RD_A(3);
        } else {
          A[0] = RD_A(0); B[4] = RD_B(4); B[5] = RD_B(5); B[6] = RD_B(6); B[7] = RD_B(7);
          A[1] = RD_A(1); B[3] = RD_B(3); A[2] = RD_A(2); B[2] = RD_B(2); A[3] = RD_A(3); B[1] = RD_B(1);
          A[4] = RD_A(4); B[0] = RD_B(0); A[5] = RD_A(5); A[6] = RD_A(6); A[7] = RD_A(7);
        }
        PIN();
#pragma unroll
        for (int qq = 0; qq < 8; qq++) {
#pragma unroll
          for (int tl = 0; tl < 4; tl++)
            if (qq <= 4 * hf + tl) acc[tl] = mfma16h(A[qq], B[4 * hf + tl - qq], acc[tl]);
          PIN();
          if (hf == 0 && qq < 2) LDW1(2 * pi + qq);
          if (hf == 1 && qq < 7) LDW1(4 + 7 * pi + qq);
        }
      }
      STAMP(17 + 2 * hf);
#pragma unroll
      for (int tl = 0; tl < 4; tl++) *(LAS f32x4 *)(ex + (pq * 4 + tl) * 1024) = acc[tl];
      if (hf == 1) asm volatile("s_waitcnt vmcnt(14) ; PUBWAIT" ::: "memory");
      BAR();
      if (hf == 1) {
        if (tid < 8) __hip_atomic_fetch_add((gu32 *)(p.ws + WS_CNT) + (0 * 128 + tid * 16 + (m & 15)) * 64, 1u, RLX_AGENT);
        csg_ = ld8(rs, cso);
      }
      const int t = 4 * hf + pq;
      const int l0 = 256 * t + l16;
      const f32x4 y0 = *(LAS f32x4 *)(ex + (0 * 4 + pq) * 1024), y1 = *(LAS f32x4 *)(ex + (1 * 4 + pq) * 1024);
      const f32x4 y2 = *(LAS f32x4 *)(ex + (2 * 4 + pq) * 1024), y3 = *(LAS f32x4 *)(ex + (3 * 4 + pq) * 1024);
      const v2u hh = *(LAS v2u *)(S + LD_U + c * U_CH + (256 + l0) * 2);
      const v2u xx = *(LAS v2u *)(S + LD_XS + c * 4096 + l0 * 2);
      const f32x4 y = (y0 + y1) + (y2 + y3);
      const h2 ha = ash2(hh.x), hb = ash2(hh.y);
      const f2 a1 = {A1, A1}, a2 = {A2, A2}, cc = {c2, c2};
      const f2 o01 = __builtin_elementwise_fma(a2, f2{(float)ha.x, (float)ha.y},
                         __builtin_elementwise_fma(a1, f2{y[0], y[1]}, f2{bflo(xx.x), bfhi(xx.x)}));
      const f2 o23 = __builtin_elementwise_fma(a2, f2{(float)hb.x, (float)hb.y},
                         __builtin_elementwise_fma(a1, f2{y[2], y[3]}, f2{bflo(xx.y), bfhi(xx.y)}));
      const u32 X0 = pkbf(o01.x, o01.y), X1 = pkbf(o23.x, o23.y);
      const f2 s01 = f2{bflo(X0), bfhi(X0)} * cc, s23 = f2{bflo(X1), bfhi(X1)} * cc;
      const u32 Y0 = pkbf(s01.x, s01.y), Y1 = pkbf(s23.x, s23.y);
      const auto r0 = __builtin_amdgcn_permlane16_swap(X0, Y0, false, false);
      const auto r1 = __builtin_amdgcn_permlane16_swap(X1, Y1, false, false);
      st16(rs, xoff + 4 * t * D * 128, v4u{r0[0], r1[0], r0[1], r1[1]});
      PIN();
      STAMP(18 + 4 * hf);
    }
#pragma unroll
    for (int i = 18; i < 24; i++) LDW1(i);
    asm volatile("s_waitcnt vmcnt(6) ; PUBWAIT" ::: "memory");
    BAR();
    if (tid < 8) __hip_atomic_fetch_add((gu32 *)(p.ws + WS_CNT) + (1 * 128 + tid * 16 + (m & 15)) * 64, 1u, RLX_AGENT);
#undef RD_A
#undef RD_B
  }
  STAMP(7);

  if (w == 0) {
    poll_counters(p, 0 * 128 + g * 16, 16, 14u, lane);
  } else {
#pragma unroll
    for (int i = 24; i < 28; i++) LDW1(i);
  }
  BAR();
  STAMP(8);
  v4u xs[7];
  const int xso = tid * 16;
  const int xlo = (tid >> 3) * 128 + (((tid & 7) ^ (((tid >> 4) & 1) << 2)) * 16);
#pragma unroll
  for (int i = 0; i < 7; i++) xs[i] = ld16s(rs, xso, WS_X1S + tt * XB_SZ + i * 8192);
  PIN();
  if (w == 0) {
#pragma unroll
    for (int i = 24; i < 28; i++) LDW1(i);
  }
  *(LAS u32 *)(S + LD_CSL + (w * 64 + lane) * 4) = finish_granule(rs, cso, tag, csg_);
#define NWA 4
#define NWB 4
  constexpr int NWPF = NWA + NWB;
  v4u wd[28];
  const int wdo = WS_WDF + (2 * dtile * 112 + 14 * w) * 1024;
#define LDWD(i) (wd[i] = ld16s(rs, lane16, wdo + (((i) / 14) * 112 + (i) % 14) * 1024))
#pragma unroll
  for (int i = 0; i < 7; i++) {
    *(LAS v4u *)(S + LD_XB + xlo + i * 8192) = xs[i];
    if (i < NWA) LDWD(i);
    PIN();
  }
  BAR();
  STAMP(9);
  v2u sg2[2];
  u32 pk;
  {
    const int G = lane >> 4, e = (lane >> 2) & 3, pp = lane & 3, h = lane >> 5;
    const int sw = ((e >> 1) & 1) * 64;
    LAS unsigned char *bx0 = S + LD_XB + (8 * h + e) * 128 + ((0 + 32 * (G & 1) + 8 * pp) ^ sw);
    LAS unsigned char *bx1 = S + LD_XB + (8 * h + e) * 128 + ((64 + 32 * (G & 1) + 8 * pp) ^ sw);
    LAS unsigned char *cl = S + LD_CSL + w * 256 + 16 * h;
    const int ho = WS_HF + phi * 1024 + lane16;
    f32x16 acc[2][2];
    f2 ep1[2], ep2[2];
    u32 hp[2][4];
#define EPI_PART(hf_, j, gq) do { \
      const f32x4 csg = *(LAS f32x4 *)(cl + 32 * (gq)), csu = *(LAS f32x4 *)(cl + 32 * (gq) + 64); \
      const f32x4 bg = *(LAS f32x4 *)(cl + 32 * (gq) + 128), bu = *(LAS f32x4 *)(cl + 32 * (gq) + 192); \
      _Pragma("unroll") for (int i = 0; i < 4; i += 2) { \
        const f2 gg = __builtin_elementwise_fma(ep1[j], f2{acc[hf_][j][4 * (gq) + i], acc[hf_][j][4 * (gq) + i + 1]}, \
                          __builtin_elementwise_fma(ep2[j], f2{csg[i], csg[i + 1]}, f2{bg[i], bg[i + 1]})); \
        const f2 uu = __builtin_elementwise_fma(ep1[j], f2{acc[hf_][j][8 + 4 * (gq) + i], acc[hf_][j][8 + 4 * (gq) + i + 1]}, \
                          __builtin_elementwise_fma(ep2[j], f2{csu[i], csu[i + 1]}, f2{bu[i], bu[i + 1]})); \
        const f2 hv = gg * uu; \
        hp[j][2 * (gq) + i / 2] = pkbf(hv.x, hv.y); \
      } } while (0)
#define EPI_STORE(hf_, j) st16(rs, ho + (2 * (16 * (hf_) + tt) + (j)) * 112 * 1024, v4u{hp[j][0], hp[j][1], hp[j][2], hp[j][3]})
#define EPI_STATS(hf_) do { \
      const int sto_ = WS_STAT2 + (64 * (16 * (hf_) + tt) + (lane & 31)) * 8; \
      const u32 sv0 = finish_granule(rs, sto_, tag, sg2[0]), sv1 = finish_granule(rs, sto_ + 256, tag, sg2[1]); \
      const float r0_ = __builtin_amdgcn_rcpf(bfhi(sv0)), r1_ = __builtin_amdgcn_rcpf(bfhi(sv1)); \
      ep1[0] = f2{r0_, r0_}; ep1[1] = f2{r1_, r1_}; \
      ep2[0] = f2{-r0_ * bflo(sv0), -r0_ * bflo(sv0)}; ep2[1] = f2{-r1_ * bflo(sv1), -r1_ * bflo(sv1)}; } while (0)
    constexpr int NREM = 28 - NWPF;
#define WDSTEP(s) (((s) * NREM / 27 != ((s) + 1) * NREM / 27 && (s) < 27) ? NWPF + (s) * NREM / 27 : -1)
    constexpr int PUBSTEP = 10, PUBCNT = (PUBSTEP + 1) * NREM / 27;
#pragma unroll
    for (int hf = 0; hf < 2; hf++) {
#pragma unroll
      for (int j = 0; j < 2; j++)
#pragma unroll
        for (int i = 0; i < 16; i++) acc[hf][j][i] = 0.f;
      v4u fb0[3], fb1[3];
      LAS unsigned char *bh0 = bx0 + hf * XB_SZ, *bh1 = bx1 + hf * XB_SZ;
      asm volatile("" : "+v"(bh0), "+v"(bh1));
#define RD_X(s) do { const int o_ = (s) * 2048; fb0[(s) % 3] = tr8(bh0 + o_, bh0 + o_ + 512); fb1[(s) % 3] = tr8(bh1 + o_, bh1 + o_ + 512); } while (0)
      RD_X(0); RD_X(1);
#pragma unroll
      for (int s = 0; s < 28; s++) {
        if (s + 2 < 28) RD_X(s + 2);
        if (hf == 0) {
          if ((s & 1) == 0 && s / 2 < NWB) LDWD(NWA + s / 2);
          if (s == 3 && w == 0) pk = peek_counters(p, 1 * 128 + g * 16, 16, lane);
          if (s == 9) {
            if (w == 0) finish_counters(p, 1 * 128 + g * 16, 16, 14u, lane, pk);
            BAR();
          }
          if (s >= 9 && s < 16) xs[s - 9] = ld16s(rs, xso, WS_X1S + (16 + tt) * XB_SZ + (s - 9) * 8192);
          if (s >= 21) *(LAS v4u *)(S + LD_XB + XB_SZ + xlo + (s - 21) * 8192) = xs[s - 21];
        } else {
          if (WDSTEP(s) >= 0) LDWD(WDSTEP(s) < 0 ? 0 : WDSTEP(s));
          if (s == PUBSTEP) {
            asm volatile("s_waitcnt vmcnt(%0) ; PUBWAIT" ::"n"(PUBCNT) : "memory");
            BAR();
            if (tid == 0) __hip_atomic_fetch_add((gu32 *)(p.ws + WS_CNT) + (256 + tt) * 64, 1u, RLX_AGENT);
          }
        }
        if (s == 20) {
          const int sto = WS_STAT2 + (64 * (16 * hf + tt) + (lane & 31)) * 8;
          sg2[0] = ld8(rs, sto); sg2[1] = ld8(rs, sto + 256);
        }
        PIN();
        acc[hf][0] = mfma32b(w1[s], fb0[s % 3], acc[hf][0]);
        acc[hf][1] = mfma32b(w1[s], fb1[s % 3], acc[hf][1]);
        PIN();
        if (s == 3) STAMP(23 + 3 * hf);
        if (s == 27) STAMP(24 + 3 * hf);
      }
#undef RD_X
      if (hf == 0) {
        EPI_STATS(0);
        EPI_PART(0, 0, 0); EPI_PART(0, 0, 1); EPI_PART(0, 1, 0); EPI_PART(0, 1, 1);
        PIN();
        EPI_STORE(0, 0); EPI_STORE(0, 1);
        PIN();
        BAR();
        STAMP(10);
      }
    }
    EPI_STATS(1);
    STAMP(28);
    EPI_PART(1, 0, 0); EPI_PART(1, 0, 1); EPI_PART(1, 1, 0); EPI_PART(1, 1, 1);
    if (w == 0) pk = peek_counters(p, 256 + (t2 >> 1), 1, lane);
    PIN();
    EPI_STORE(1, 0); EPI_STORE(1, 1);
    PIN();
    STAMP(11);
#undef EPI_PART
#undef EPI_STORE
#undef EPI_STATS
  }

  if (w == 0) finish_counters(p, 256 + (t2 >> 1), 1, 14u, lane, pk);
  BAR();
  STAMP(12);
  v4u gbg[2], hb[14], res[2];
  const int gbo = WS_MODG + (5 * D + 64 * dtile + 4 * (2 * w + (lane >> 5))) * 8;
  const int reso = WS_X1T + ((t2 >> 1) * D + 64 * dtile + ((tid & 255) >> 2)) * 128 + (t2 & 1) * 64 + (tid & 3) * 16;
  const int hbo = WS_HF + (t2 * 112 + 14 * w) * 1024;
#pragma unroll
  for (int k = 0; k < 5; k++) hb[k] = ld16s(rs, lane16, hbo + k * 1024);
  PIN();
  asm volatile("s_waitcnt vmcnt(5) ; PUBWAIT" ::: "memory");
  BAR();
  if (tid == 0) __hip_atomic_fetch_add((gu32 *)(p.ws + WS_CNT) + (256 + 16 + tt) * 64, 1u, RLX_AGENT);
#pragma unroll
  for (int k = 5; k < 14; k++) hb[k] = ld16s(rs, lane16, hbo + k * 1024);
  res[0] = ld16s(rs, reso, 0); res[1] = ld16s(rs, reso, 16 * D * 128);
  gbg[0] = ld16(rs, gbo); gbg[1] = ld16(rs, gbo + 16);
  PIN();
#pragma unroll
  for (int hf = 0; hf < 2; hf++) {
    f32x16 acc[2];
#pragma unroll
    for (int j = 0; j < 2; j++)
#pragma unroll
      for (int i = 0; i < 16; i++) acc[j][i] = 0.f;
#pragma unroll
    for (int k = 0; k < 14; k++) {
      acc[0] = mfma32b(wd[k], hb[k], acc[0]);
      acc[1] = mfma32b(wd[14 + k], hb[k], acc[1]);
      if (hf == 0 && k == 9 && w == 0) { PIN(); pk = peek_counters(p, 256 + 16 + (t2 >> 1), 1, lane); PIN(); }
    }
    PIN();
    STAMP(29 + hf);
#define LDHL(k) do { if (hf == 0) { hb[k] = ld16s(rs, lane16, hbo + (32 * 112 + (k)) * 1024); PIN(); } } while (0)
    if (hf == 0) {
      if (w == 0) finish_counters(p, 256 + 16 + (t2 >> 1), 1, 14u, lane, pk);
      BAR();
    }
    {
      const int col = lane & 31, h = lane >> 5;
      LAS unsigned char *rd = S + LD_RED + w * RED_W + col * RED_ROW + h * 16;
#pragma unroll
      for (int i = 0; i < 8; i++) {
        const int dl = i >> 2, gq = i & 3;
        *(LAS f32x4 *)(rd + dl * 128 + gq * 32) = f32x4{acc[dl][4 * gq], acc[dl][4 * gq + 1], acc[dl][4 * gq + 2], acc[dl][4 * gq + 3]};
        PIN();
        LDHL(i);
      }
      if (tid < 256) *(LAS v4u *)(S + LD_RES + (tid >> 2) * 64 + (tid & 3) * 16) = res[hf];
    }
    if (hf == 0) {
#pragma unroll 1
      for (unsigned sp = 0; sp < SPIN_CAP; sp++) {
        if (__all(gbg[0].y == tag && gbg[0].w == tag && gbg[1].y == tag && gbg[1].w == tag)) break;
        gbg[0] = ld16(rs, gbo); gbg[1] = ld16(rs, gbo + 16);
      }
    }
    BAR();
    {
      const int tok = lane & 31, dq = 2 * w + (lane >> 5);
      f32x4 part[8];
#pragma unroll
      for (int ww = 0; ww < 8; ww++) part[ww] = *(LAS f32x4 *)(S + LD_RED + ww * RED_W + tok * RED_ROW + dq * 16);
      const v2u rr = trread(S + LD_RES + (4 * dq + ((lane >> 2) & 3)) * 64 + (16 * ((lane >> 4) & 1) + 4 * (lane & 3)) * 2);
      PIN();
      LDHL(8); LDHL(9);
      const f32x4 s0 = (part[0] + part[1]) + (part[2] + part[3]);
      PIN();
      LDHL(10); LDHL(11);
      const f32x4 sum = s0 + ((part[4] + part[5]) + (part[6] + part[7]));
      PIN();
      LDHL(12); LDHL(13);
      const u32 b01 = pkbf(sum[0], sum[1]), b23 = pkbf(sum[2], sum[3]);
      const u32 t01 = pkbf(asf(gbg[0].x) * bflo(b01), asf(gbg[0].z) * bfhi(b01));
      const u32 t23 = pkbf(asf(gbg[1].x) * bflo(b23), asf(gbg[1].z) * bfhi(b23));
      const float o0 = bflo(rr.x) + bflo(t01), o1 = bfhi(rr.x) + bfhi(t01);
      const float o2 = bflo(rr.y) + bflo(t23), o3 = bfhi(rr.y) + bfhi(t23);
      *(GAS v2u *)((GAS char *)p.out + (u32)((32 * (32 * hf + t2) + tok) * D + 64 * dtile + 4 * dq) * 2u) = v2u{pkbf(o0, o1), pkbf(o2, o3)};
    }
#undef LDHL
    STAMP(13 + 2 * hf);
    if (hf == 0) BAR();
  }
#undef LDW1
}

__device__ __forceinline__ void side_role(const Params &p, LAS unsigned char *S, const rsrc_t rs, const int tid,
                                          const int lane, const int w, const int b, const u32 epoch) {
  const int g = b & 7, r = b >> 3, sidx = 4 * g + r - 28;
  const int sc = sidx < 28 ? sidx : 27;
  const int lane16 = lane * 16;
  const int tau = 16 * g + (r >> 1);
  const u32 xo = (u32)(16 * tau + 2 * w) * (2u * D) + (u32)(lane < 56 ? lane : 55) * 16u;
  const v4u x0 = gld<v4u>(p.x, xo), x1 = gld<v4u>(p.x, xo + 2u * D);
  const u32 tag = __builtin_amdgcn_readfirstlane(epoch) + 1u;
  if (tid == 0) st4(rs, WS_WRDY + (224 + sidx) * 16, tag);
  if ((r & 1) == 0) {
    ln1_rows(S, x0, x1, w, lane);
    STAMP(1);
    BAR();
    publish_xt(S, rs, lane, w, tau, tag);
  }
  STAMP(3);
  {
    const int dd = tid < D ? tid : 0;
    const int o1 = WS_WRDY + (tid < 224 ? tid : 0) * 16, o2 = WS_MODG + (4 * D + dd) * 8, o3 = WS_MODG + (3 * D + dd) * 8;
    const v2u g2 = ld8(rs, o2), g3 = ld8(rs, o3);
    const u32 vcb = finish_granule(rs, o2, tag, g2), vsb = finish_granule(rs, o3, tag, g3);
    if (tid < D) {
      ((LAS u16 *)(S + LD_CV))[tid] = (u16)(pkbf(1.0f + asf(vcb), 0.f) & 0xFFFFu);
      ((LAS u16 *)(S + LD_CV))[D + tid] = (u16)(vsb >> 16);
    }
    if (w < 4) {
#pragma unroll 1
      for (unsigned sp = 0; sp < SPIN_CAP; sp++) {
        const u32 wr = __builtin_amdgcn_raw_buffer_load_b32(rs, o1, 0, SC1);
        if (__all(wr == tag)) break;
        __builtin_amdgcn_s_sleep(4);
      }
      __builtin_amdgcn_fence(__ATOMIC_ACQUIRE, "wavefront");
    }
  }
  BAR();
  STAMP(5);
  {
    const int ph = 4 * sc + (w >> 1), s0 = 14 * (w & 1);
    v4u wf[14];
#pragma unroll
    for (int k = 0; k < 14; k++) wf[k] = ld16s(rs, lane16, WS_W1F + (ph * 28 + s0 + k) * 1024);
    PIN();
    f32x16 acc;
#pragma unroll
    for (int i = 0; i < 16; i++) acc[i] = 0.f;
    LAS unsigned char *cvp = S + LD_CV + ((lane & 31) == 1 ? D * 2 : 0) + (lane >> 5) * 16;
#pragma unroll
    for (int k = 0; k < 14; k++) acc = mfma32b(wf[k], *(LAS v4u *)(cvp + (s0 + k) * 32), acc);
    LAS float *pr = (LAS float *)(S) + w * 64;
    if ((lane & 31) < 2) {
#pragma unroll
      for (int i = 0; i < 16; i++) pr[(lane & 31) * 32 + (i & 3) + 8 * (i >> 2) + 4 * (lane >> 5)] = acc[i];
    }
  }
  BAR();
  if (sidx < 28 && tid < 256) {
    const int pl = tid >> 6, e = tid & 63;
    const float v = ((LAS float *)S)[(2 * pl) * 64 + e] + ((LAS float *)S)[(2 * pl + 1) * 64 + e];
    st8(rs, WS_CSB + ((4 * sidx + pl) * 64 + e) * 8, v2u{__builtin_bit_cast(u32, v), tag});
  }
  STAMP(6);
#pragma unroll 1
  for (int hf = 0; hf < 2; hf++) {
    if (w == 0) poll_counters(p, hf * 128 + g * 16, 16, 14u, lane);
    BAR();
    const int tau2 = 32 * hf + sidx;
    const int tp = lane & 15, rgp = (lane >> 4) + 4 * w;
    u32 xv[14];
#pragma unroll
    for (int i = 0; i < 14; i++)
      xv[i] = __builtin_amdgcn_raw_buffer_load_b32(rs, rgp * 128 + tp * 4, WS_X1T + ((tau2 >> 1) * D + 32 * i) * 128 + (tau2 & 1) * 64, SC1);
    PIN();
    f2 sm = {0.f, 0.f}, sq = {0.f, 0.f};
#pragma unroll
    for (int i = 0; i < 14; i++) {
      const f2 a = {bflo(xv[i]), bfhi(xv[i])};
      sm += a; sq = __builtin_elementwise_fma(a, a, sq);
    }
    LAS f32x4 *pr = (LAS f32x4 *)(S + 4096);
    pr[rgp * 16 + tp] = f32x4{sm.x, sm.y, sq.x, sq.y};
    BAR();
    if (tid < 128) {
      const int tok = tid >> 2, part = tid & 3;
      LAS float *pf = (LAS float *)pr + (part * 8 * 16 + (tok >> 1)) * 4 + (tok & 1);
      float s = 0.f, q = 0.f;
#pragma unroll
      for (int i = 0; i < 8; i++) { s += pf[i * 64]; q += pf[i * 64 + 2]; }
      s += dpp0<0xB1, 0xF>(s); q += dpp0<0xB1, 0xF>(q);
      s += dpp0<0x4E, 0xF>(s); q += dpp0<0x4E, 0xF>(q);
      const float mean = s * (1.0f / D);
      const float var = rbf(fmaxf(q * (1.0f / D) - mean * mean, 0.f));
      const float sd = rbf(__builtin_amdgcn_sqrtf(rbf(var + 1e-5f)));
      if (part == 0) st8(rs, WS_STAT2 + (32 * tau2 + tok) * 8, v2u{pkbf(mean, sd), tag});
    }
    STAMP(8 + hf);
  }
}

__global__ void k_mark(unsigned long long *q) { if (threadIdx.x == 0) *q = __builtin_amdgcn_s_memrealtime(); }
__global__ void __launch_bounds__(512, 2) k_dummy(unsigned long long *q) {
  const unsigned long long t_entry = __builtin_amdgcn_s_memrealtime();
  extern __shared__ char dsm[];

  if (blockIdx.x == 0 && threadIdx.x < 64) {
    const unsigned long long tm = *(volatile unsigned long long *)q;
    const unsigned long long t1 = __builtin_amdgcn_s_memrealtime(), d = (t_entry - tm) * 0;
    while (__builtin_amdgcn_s_memrealtime() - t1 < d && d < 100000) __builtin_amdgcn_s_sleep(1);
  }

}
__global__ void __launch_bounds__(NTHR, 2) k_main(Params p) {
  extern __shared__ __attribute__((aligned(16))) unsigned char smem[];
  LAS unsigned char *S = (LAS unsigned char *)smem;
  const int tid = threadIdx.x, lane = tid & 63, b = blockIdx.x;
  const int w = __builtin_amdgcn_readfirstlane(tid >> 6);
  const rsrc_t rs = __builtin_amdgcn_make_buffer_rsrc((void *)p.ws, 0, WS_END, 0x00020000);
  u32 pf;
  {
    unsigned long long pc;
    asm volatile("s_getpc_b64 %0" : "=s"(pc));
    pf = gld<u32>((const void *)(pc & ~127ull), (u32)(tid < PF_LINES ? tid : 0) * 128u);
  }
  const u32 epoch = __builtin_amdgcn_raw_buffer_load_b32(rs, WS_EPOCH, 0, SC1);
  STAMP(0);
  if ((b >> 3) < 28) main_role(p, S, rs, tid, lane, w, b, epoch);
  else side_role(p, S, rs, tid, lane, w, b, epoch);
  asm volatile("" ::"v"(pf));
}

}

extern "C" void kernel_launch(void *const *d_in, const int *in_sizes, int n_in, void *d_out, int out_size, void *d_ws,
                              size_t ws_size, hipStream_t stream) {
  using namespace mk;
  Params p;
  p.x = (const u16 *)d_in[0];
  p.Are = (const u16 *)d_in[1]; p.Aim = (const u16 *)d_in[2];
  p.Bre = (const u16 *)d_in[3]; p.Bim = (const u16 *)d_in[4];
  p.Cre = (const u16 *)d_in[5]; p.Cim = (const u16 *)d_in[6];
  p.logdt = (const u16 *)d_in[7]; p.Dsk = (const u16 *)d_in[8];
  p.wg = (const u16 *)d_in[10]; p.wu = (const u16 *)d_in[11]; p.wd = (const u16 *)d_in[12];
  p.cvec = (const u16 *)d_in[13]; p.wmod = (const u16 *)d_in[14];
  p.out = (u16 *)d_out;
  p.ws = (unsigned char *)d_ws;
  (void)hipFuncSetAttribute((const void *)k_main, hipFuncAttributeMaxDynamicSharedMemorySize, LDS_BYTES);
  k_mark<<<1, 64, 0, stream>>>((unsigned long long *)(p.ws + WS_DBG));
  (void)hipFuncSetAttribute((const void *)k_dummy, hipFuncAttributeMaxDynamicSharedMemorySize, 163840);
  k_dummy<<<256, 512, 163840, stream>>>((unsigned long long *)(p.ws + WS_DBG));
  k_main<<<NWG, NTHR, LDS_BYTES, stream>>>(p);
}
```

```cpp
#include <hip/hip_runtime.h>
#include <stdint.h>

namespace mk {

typedef unsigned short u16;
typedef unsigned u32;
typedef u32 v4u __attribute__((ext_vector_type(4)));
typedef u32 v2u __attribute__((ext_vector_type(2)));
typedef float f2 __attribute__((ext_vector_type(2)));
typedef float f32x4 __attribute__((ext_vector_type(4)));
typedef float f32x16 __attribute__((ext_vector_type(16)));
typedef _Float16 h2 __attribute__((ext_vector_type(2)));
typedef _Float16 h8 __attribute__((ext_vector_type(8)));
typedef __bf16 b2 __attribute__((ext_vector_type(2)));
typedef __bf16 b8 __attribute__((ext_vector_type(8)));
typedef short s4 __attribute__((ext_vector_type(4)));
#define LAS __attribute__((address_space(3)))
#define GAS __attribute__((address_space(1)))
typedef GAS u32 gu32;
#define RLX_AGENT __ATOMIC_RELAXED, __HIP_MEMORY_SCOPE_AGENT

constexpr int L = 2048, D = 448, NS = 256, F = 1792;
constexpr int NWG = 256, NTHR = 512;
constexpr int SC1 = 16;
constexpr int PF_LINES = 190;

constexpr int WS_EPOCH = 0;
constexpr int WS_CNT = 4096;
constexpr int NCNT = 288;
constexpr int WS_STAT1 = WS_CNT + NCNT * 256;
constexpr int WS_MODG = WS_STAT1 + 2048 * 8;
constexpr int WS_CSB = WS_MODG + 2688 * 8;
constexpr int WS_STAT2 = WS_CSB + 112 * 64 * 8;
constexpr int WS_WRDY = WS_STAT2 + 2048 * 8;
constexpr int WS_XG = WS_WRDY + 256 * 16;
constexpr int WS_W1F = WS_XG + 128 * 224 * 128;
constexpr int WS_WDF = WS_W1F + 112 * 28 * 1024;
constexpr int WS_X1T = WS_WDF + 14 * 112 * 1024;
constexpr int WS_X1S = WS_X1T + 32 * 448 * 128;
constexpr int WS_HF = WS_X1S + 32 * 448 * 128;
constexpr int WS_DBG = WS_HF + 64 * 112 * 1024;
constexpr int WS_END = WS_DBG + (8 << 20);
static_assert(WS_STAT1 % 128 == 0 && WS_MODG % 128 == 0 && WS_CSB % 128 == 0 && WS_STAT2 % 128 == 0 && WS_XG % 128 == 0 && WS_W1F % 128 == 0, "align");

constexpr int PQ_ROW = 1056, PQ_CH = 57 * PQ_ROW;
constexpr int LD_PQ = 0;
constexpr int LD_KC = 0;
constexpr int KC_CP = 4160, KC_CH = 4 * KC_CP;
constexpr int LD_EX = 33280;
constexpr int LD_XB = 0;
constexpr int XB_SZ = 448 * 128;
constexpr int LD_RED = 0;
constexpr int RED_ROW = 272, RED_W = 32 * RED_ROW;
constexpr int LD_RES = 8 * RED_W;
constexpr int LD_KD = 120384;
constexpr int KD_CH = 784 * 2;
constexpr int LD_U = LD_KD + 2 * KD_CH;
constexpr int U_CH = 2304 * 2;
constexpr int LD_XS = LD_U + 2 * U_CH;
constexpr int LD_ST1 = 140928;
constexpr int LD_XROW = 120384;
constexpr int LD_WSTG = LD_XROW + 16128;
constexpr int WSTG_ROW = 464, WSTG_M = 16 * WSTG_ROW;
constexpr int LD_SMALL = LD_WSTG + 3 * WSTG_M;
constexpr int LD_STAT = LD_SMALL;
constexpr int LD_WMAX = LD_SMALL + 64;
constexpr int LD_CSL = LD_SMALL + 96;
constexpr int LD_STAMP = LD_CSL + 2048;
constexpr int LD_CV = LD_STAMP + 256;
constexpr int LD_END = LD_CV + 2 * 448 * 2;
constexpr int LDS_BYTES = 160 * 1024;
static_assert(LD_XS + 8192 <= LD_ST1 && LD_ST1 + 16384 <= LD_SMALL && LD_XS + 8192 <= LD_SMALL && LD_END <= LDS_BYTES && LD_CSL % 16 == 0 && LD_CV % 16 == 0, "lds");
static_assert(LD_RES + 4096 <= LD_KD, "lds red");

struct Params {
  const u16 *x, *Are, *Aim, *Bre, *Bim, *Cre, *Cim, *logdt, *Dsk, *wg, *wu, *wd, *cvec, *wmod;
  u16 *out;
  unsigned char *ws;
};

struct KTab { u16 v[256][16]; };
constexpr int bf16_round_int(int j) {
  if (j < 256) return j;
  int sh = 0; int t = j; while (t >= 256) { t >>= 1; sh++; }
  int q = j >> sh, rem = j & ((1 << sh) - 1), half = 1 << (sh - 1);
  if (rem > half || (rem == half && (q & 1))) q++;
  return q << sh;
}
constexpr int kd_index(int j) {
  if (j < 0) return 768;
  int l = bf16_round_int(j);
  if (l >= 2048) return 40 * 16;
  int k = l < 256 ? 0 : (l < 512 ? 1 : (l < 1024 ? 2 : 3));
  int base = k ? (128 << k) : 0;
  int rem = l - base;
  int a = rem >> 7, b = (rem >> 3) & 15, c = (rem & 7) >> k;
  int row = (k ? 8 + 8 * k : 0) + a * (8 >> k) + c;
  return row * 16 + b;
}
constexpr KTab make_ktab() {
  KTab t{};
  for (int i = 0; i < 256; i++)
    for (int j = 0; j < 16; j++) t.v[i][j] = (u16)(2 * (j < 11 ? kd_index(8 * i - 3 + j) : 768));
  return t;
}
__device__ const KTab g_ktab = make_ktab();

__device__ __forceinline__ float asf(u32 v) { return __builtin_bit_cast(float, v); }
__device__ __forceinline__ h2 ash2(u32 v) { return __builtin_bit_cast(h2, v); }
__device__ __forceinline__ float bf2f(u32 v) { return __builtin_bit_cast(float, v << 16); }
__device__ __forceinline__ float bflo(u32 v) { return __builtin_bit_cast(float, v << 16); }
__device__ __forceinline__ float bfhi(u32 v) { return __builtin_bit_cast(float, v & 0xFFFF0000u); }
__device__ __forceinline__ u32 pkbf(float a, float b) {
  return __builtin_bit_cast(u32, __builtin_convertvector(f2{a, b}, b2));
}
__device__ __forceinline__ u32 pkh(float a, float b) {
  return __builtin_bit_cast(u32, __builtin_convertvector(f2{a, b}, h2));
}
__device__ __forceinline__ float rbf(float a) { return bflo(pkbf(a, a)); }
__device__ __forceinline__ f2 cmul(f2 a, f2 b) {
  f2 r;
  asm("v_pk_mul_f32 %0, %1, %2 op_sel_hi:[0,1]\n\t"
      "v_pk_fma_f32 %0, %1, %2, %0 op_sel:[1,1,0] op_sel_hi:[1,0,1] neg_lo:[1,0,0]"
      : "=&v"(r) : "v"(a), "v"(b));
  return r;
}
__device__ __forceinline__ float dot2bf(u32 a, u32 b, float c) {
  return __builtin_amdgcn_fdot2_f32_bf16(__builtin_bit_cast(b2, a), __builtin_bit_cast(b2, b), c, false);
}
template <class T> __device__ __forceinline__ T gld(const void *base, u32 off) { return *(const GAS T *)((const GAS char *)base + off); }
template <int CTRL, int RM> __device__ __forceinline__ float dpp0(float v) {
  return __builtin_bit_cast(float, __builtin_amdgcn_update_dpp(0, __builtin_bit_cast(int, v), CTRL, RM, 0xF, true));
}
__device__ __forceinline__ float wave_sum(float v) {
  v += dpp0<0x111, 0xF>(v);
  v += dpp0<0x112, 0xF>(v);
  v += dpp0<0x114, 0xF>(v);
  v += dpp0<0x118, 0xF>(v);
  v += dpp0<0x142, 0xA>(v);
  v += dpp0<0x143, 0xC>(v);
  return __builtin_bit_cast(float, __builtin_amdgcn_readlane(__builtin_bit_cast(int, v), 63));
}
__device__ __forceinline__ float wave_max_nonneg(float v) {
  v = fmaxf(v, dpp0<0x111, 0xF>(v));
  v = fmaxf(v, dpp0<0x112, 0xF>(v));
  v = fmaxf(v, dpp0<0x114, 0xF>(v));
  v = fmaxf(v, dpp0<0x118, 0xF>(v));
  v = fmaxf(v, dpp0<0x142, 0xA>(v));
  v = fmaxf(v, dpp0<0x143, 0xC>(v));
  return __builtin_bit_cast(float, __builtin_amdgcn_readlane(__builtin_bit_cast(int, v), 63));
}
__device__ __forceinline__ v2u trread(LAS unsigned char *p) {
  return __builtin_bit_cast(v2u, __builtin_amdgcn_ds_read_tr16_b64_v4i16((LAS s4 *)p));
}
__device__ __forceinline__ v4u tr8(LAS unsigned char *p0, LAS unsigned char *p1) {
  v2u a = trread(p0), b = trread(p1);
  return v4u{a.x, a.y, b.x, b.y};
}
__device__ __forceinline__ f32x4 mfma16h(v4u a, v4u b, f32x4 c) {
  return __builtin_amdgcn_mfma_f32_16x16x32_f16(__builtin_bit_cast(h8, a), __builtin_bit_cast(h8, b), c, 0, 0, 0);
}
__device__ __forceinline__ f32x16 mfma32b(v4u a, v4u b, f32x16 c) {
  return __builtin_amdgcn_mfma_f32_32x32x16_bf16(__builtin_bit_cast(b8, a), __builtin_bit_cast(b8, b), c, 0, 0, 0);
}
#define BAR() asm volatile("s_waitcnt lgkmcnt(0)\n\ts_barrier" ::: "memory")
#define DRAIN() asm volatile("s_waitcnt vmcnt(0)" ::: "memory")
#define PIN() do { asm volatile("" ::: "memory"); __builtin_amdgcn_sched_barrier(0); } while (0)
typedef __amdgpu_buffer_rsrc_t rsrc_t;
__device__ __forceinline__ v4u ld16(rsrc_t rs, int off) { return __builtin_amdgcn_raw_buffer_load_b128(rs, off, 0, SC1); }
__device__ __forceinline__ v4u ld16s(rsrc_t rs, int voff, int soff) { return __builtin_amdgcn_raw_buffer_load_b128(rs, voff, soff, SC1); }
__device__ __forceinline__ void st16s(rsrc_t rs, int voff, int soff, v4u v) { __builtin_amdgcn_raw_buffer_store_b128(v, rs, voff + soff, 0, SC1); }
__device__ __forceinline__ v2u ld8(rsrc_t rs, int off) { return __builtin_amdgcn_raw_buffer_load_b64(rs, off, 0, SC1); }
__device__ __forceinline__ void st16(rsrc_t rs, int off, v4u v) { __builtin_amdgcn_raw_buffer_store_b128(v, rs, off, 0, SC1); }
__device__ __forceinline__ void st8(rsrc_t rs, int off, v2u v) { __builtin_amdgcn_raw_buffer_store_b64(v, rs, off, 0, SC1); }
__device__ __forceinline__ void st4(rsrc_t rs, int off, u32 v) { __builtin_amdgcn_raw_buffer_store_b32(v, rs, off, 0, SC1); }

constexpr unsigned SPIN_CAP = 1u << 14;

#define STAMP(i)

__device__ __forceinline__ u32 peek_counters(const Params &p, int idx0, int ns, int lane) {
  return __hip_atomic_load((gu32 *)(p.ws + WS_CNT) + (idx0 + (lane < ns ? lane : 0)) * 64, RLX_AGENT);
}
__device__ __forceinline__ void finish_counters(const Params &p, int idx0, int ns, u32 want, int lane, u32 v) {
#pragma unroll 1
  for (unsigned sp = 0; sp < SPIN_CAP; sp++) {
    if (__all(v >= want)) break;
    __builtin_amdgcn_s_sleep(1);
    v = peek_counters(p, idx0, ns, lane);
  }
  __builtin_amdgcn_fence(__ATOMIC_ACQUIRE, "wavefront");
}
__device__ __forceinline__ void poll_counters(const Params &p, int idx0, int ns, u32 want, int lane) {
  finish_counters(p, idx0, ns, want, lane, peek_counters(p, idx0, ns, lane));
}
__device__ __forceinline__ u32 finish_granule(rsrc_t rs, int off, u32 tag, v2u g) {
#pragma unroll 1
  for (unsigned sp = 0; sp < SPIN_CAP; sp++) {
    if (__all(g.y == tag)) break;
    __builtin_amdgcn_s_sleep(1);
    g = ld8(rs, off);
  }
  __builtin_amdgcn_fence(__ATOMIC_ACQUIRE, "wavefront");
  return g.x;
}

template <int ROW> __device__ __forceinline__ void pqw4(u32 m0v, f2 a, f2 b, f2 c, f2 d) {
  const u32 h0 = pkh(a.x, a.y), h1 = pkh(b.x, b.y), h2_ = pkh(c.x, c.y), h3 = pkh(d.x, d.y);
  asm volatile("s_mov_b32 m0, %4\n\ts_nop 0\n\tds_write_addtid_b32 %0 offset:%c5\n\tds_write_addtid_b32 %1 offset:%c6\n\t"
               "ds_write_addtid_b32 %2 offset:%c7\n\tds_write_addtid_b32 %3 offset:%c8"
               ::"v"(h0), "v"(h1), "v"(h2_), "v"(h3), "s"(m0v), "i"(ROW * PQ_ROW), "i"((ROW + 1) * PQ_ROW),
               "i"((ROW + 2) * PQ_ROW), "i"((ROW + 3) * PQ_ROW) : "memory");
}
template <int ROW> __device__ __forceinline__ void pqw1(u32 m0v, f2 a) {
  const u32 h0 = pkh(a.x, a.y);
  asm volatile("s_mov_b32 m0, %1\n\ts_nop 0\n\tds_write_addtid_b32 %0 offset:%c2" ::"v"(h0), "s"(m0v), "i"(ROW * PQ_ROW) : "memory");
}
__device__ __forceinline__ f2 zpow(float al, float rv) {
  const float e = __builtin_amdgcn_exp2f(al);
  const float r = __builtin_amdgcn_fractf(rv);
  return f2{e * __builtin_amdgcn_cosf(r), e * __builtin_amdgcn_sinf(r)};
}
__device__ __forceinline__ float pow2b(int e) { return asf((u32)e << 23); }

__device__ __forceinline__ void ln1_rows(LAS unsigned char *S, v4u x0, v4u x1, int w, int lane) {
  constexpr u32 ONES = 0x3F803F80u;
  float v[4];
  v[0] = dot2bf(x0.w, ONES, dot2bf(x0.z, ONES, dot2bf(x0.y, ONES, dot2bf(x0.x, ONES, 0.f))));
  v[1] = dot2bf(x0.w, x0.w, dot2bf(x0.z, x0.z, dot2bf(x0.y, x0.y, dot2bf(x0.x, x0.x, 0.f))));
  v[2] = dot2bf(x1.w, ONES, dot2bf(x1.z, ONES, dot2bf(x1.y, ONES, dot2bf(x1.x, ONES, 0.f))));
  v[3] = dot2bf(x1.w, x1.w, dot2bf(x1.z, x1.z, dot2bf(x1.y, x1.y, dot2bf(x1.x, x1.x, 0.f))));
  if (lane < 56) {
    *(LAS v4u *)(S + LD_XROW + (2 * w) * 960 + lane * 16) = x0;
    *(LAS v4u *)(S + LD_XROW + (2 * w + 1) * 960 + lane * 16) = x1;
  }
#pragma unroll
  for (int i = 0; i < 4; i++) v[i] = lane < 56 ? v[i] : 0.f;
#pragma unroll
  for (int i = 0; i < 4; i++) v[i] += dpp0<0x111, 0xF>(v[i]);
#pragma unroll
  for (int i = 0; i < 4; i++) v[i] += dpp0<0x112, 0xF>(v[i]);
#pragma unroll
  for (int i = 0; i < 4; i++) v[i] += dpp0<0x114, 0xF>(v[i]);
#pragma unroll
  for (int i = 0; i < 4; i++) v[i] += dpp0<0x118, 0xF>(v[i]);
#pragma unroll
  for (int i = 0; i < 4; i++) v[i] += dpp0<0x142, 0xA>(v[i]);
#pragma unroll
  for (int i = 0; i < 4; i++) v[i] += dpp0<0x143, 0xC>(v[i]);
  const float s1 = asf(__builtin_amdgcn_readlane(__builtin_bit_cast(int, v[2]), 63));
  const float q1 = asf(__builtin_amdgcn_readlane(__builtin_bit_cast(int, v[3]), 63));
  const float s = lane == 62 ? s1 : v[0], q = lane == 62 ? q1 : v[1];
  const float mean = s * (1.0f / D);
  const float var = rbf(fmaxf(q * (1.0f / D) - mean * mean, 0.f));
  const float sd = rbf(__builtin_amdgcn_sqrtf(rbf(var + 1e-5f)));
  if (lane >= 62) *(LAS u32 *)(S + LD_STAT + (2 * w + 63 - lane) * 4) = pkbf(mean, sd);
}
__device__ __forceinline__ void publish_xt(LAS unsigned char *S, const rsrc_t rs, int lane, int w, int tau, u32 tag) {
  const int G = lane >> 4, e = (lane >> 2) & 3, pp = lane & 3;
  LAS unsigned char *ax = S + LD_XROW + (4 * G + e) * 960 + 4 * pp * 2 + w * 32;
  const int go = WS_XG + tau * (224 * 128) + w * 1024 + (lane & 15) * 64 + G * 16;
  const v2u v0 = trread(ax), v1 = trread(ax + 256), v2 = trread(ax + 512), v3 = trread(ax + (w < 4 ? 768 : 512));
  st16(rs, go, v4u{v0.x, tag, v0.y, tag});
  st16(rs, go + 8192, v4u{v1.x, tag, v1.y, tag});
  st16(rs, go + 16384, v4u{v2.x, tag, v2.y, tag});
  if (w < 4) st16(rs, go + 24576, v4u{v3.x, tag, v3.y, tag});
  if (w == 7 && lane < 16) st8(rs, WS_STAT1 + (16 * tau + lane) * 8, v2u{*(LAS u32 *)(S + LD_STAT + lane * 4), tag});
}

__device__ __forceinline__ void main_role(const Params &p, LAS unsigned char *S, const rsrc_t rs, const int tid,
                                          const int lane, const int w, const int b, const u32 epoch) {
  const int g = b & 7, r = b >> 3, m = 28 * g + r;
  const int cch = w >> 2, t8 = tid & 255, dch = 2 * m + cch;
  const int lane16 = lane * 16;
  const bool xprod = (r & 1) == 0;
  const int tau = 16 * g + (r >> 1), mi = 14 * g + (r >> 1);
  const u32 tm = tid < 504 ? tid : 503, mrow = (tm * 21846u) >> 16, mpc = tm - 3 * mrow;
  v4u rl[3];
  u32 cv[3];
  {
    const void *rb = xprod ? (const void *)p.x : (const void *)p.wmod;
    const u32 lo = xprod ? (u32)(lane < 56 ? lane : 55) * 16u : mrow * (12u * D) + mpc * 16u;
    const u32 u0 = xprod ? (u32)(16 * tau + 2 * w) * (2u * D) : (u32)mi * 48u;
    const u32 st = xprod ? 2u * D : 168u * 12u * D;
    const bool v2 = xprod || mrow < 112;
    rl[0] = gld<v4u>(rb, lo + u0);
    rl[1] = gld<v4u>(rb, lo + u0 + st);
    rl[2] = gld<v4u>(rb, lo + u0 + (xprod ? st : (v2 ? 2 * st : 0u)));
    cv[0] = gld<u16>(p.cvec, mrow * 2u);
    cv[1] = gld<u16>(p.cvec, mrow * 2u + 336u);
    cv[2] = gld<u16>(p.cvec, (mrow < 112 ? mrow : 111u) * 2u + 672u);
  }
  PIN();
  const u32 po = (u32)(dch * NS + t8) * 2u;
  const u32 pAre = gld<u16>(p.Are, po), pAim = gld<u16>(p.Aim, po), pBre = gld<u16>(p.Bre, po), pBim = gld<u16>(p.Bim, po);
  const u32 pCre = gld<u16>(p.Cre, po), pCim = gld<u16>(p.Cim, po);
  const u32 sLdt = *(const __attribute__((address_space(4))) u32 *)(p.logdt + 2 * m);
  const u32 sDsk = *(const __attribute__((address_space(4))) u32 *)(p.Dsk + 2 * m);
  const u32 pLdt = cch ? sLdt >> 16 : sLdt & 0xFFFFu, pDsk = cch ? sDsk >> 16 : sDsk & 0xFFFFu;
  const u32 tc = tid < 448 ? tid : 447, srow = (tc * 2341u) >> 16, scol = tc - 28 * srow;
  const v4u ws0 = gld<v4u>(p.wg, srow * (2u * F) + scol * 16u + (u32)(16 * (m >> 3) * F + 224 * (m & 7)) * 2u);
  const v4u ws1 = gld<v4u>(p.wu, srow * (2u * F) + scol * 16u + (u32)(16 * (m >> 3) * F + 224 * (m & 7)) * 2u);
  const v4u ws2 = gld<v4u>(p.wd, srow * (2u * D) + scol * 16u + (u32)(16 * (m >> 1) * D + 224 * (m & 1)) * 2u);
  PIN();
  asm volatile("" : "+v"(rl[0]), "+v"(rl[1]), "+v"(rl[2]));
  const u32 tag = __builtin_amdgcn_readfirstlane(epoch) + 1u;

  if (xprod) {
    ln1_rows(S, rl[0], rl[1], w, lane);
  } else {
    f2 a[4] = {f2{0.f, 0.f}, f2{0.f, 0.f}, f2{0.f, 0.f}, f2{0.f, 0.f}};
#pragma unroll
    for (int i = 0; i < 3; i++) {
      const float cx = bf2f(cv[i]);
      float sl = rbf(cx * __builtin_amdgcn_rcpf(1.0f + __builtin_amdgcn_exp2f(cx * -1.44269504088896f)));
      if (i == 2) sl = mrow < 112 ? sl : 0.f;
      const f2 s2 = {sl, sl};
      a[0] = __builtin_elementwise_fma(s2, f2{bflo(rl[i].x), bfhi(rl[i].x)}, a[0]);
      a[1] = __builtin_elementwise_fma(s2, f2{bflo(rl[i].y), bfhi(rl[i].y)}, a[1]);
      a[2] = __builtin_elementwise_fma(s2, f2{bflo(rl[i].z), bfhi(rl[i].z)}, a[2]);
      a[3] = __builtin_elementwise_fma(s2, f2{bflo(rl[i].w), bfhi(rl[i].w)}, a[3]);
    }
    if (tid < 504) {
      LAS f32x4 *mp = (LAS f32x4 *)(S + LD_XROW + tm * 32);
      mp[0] = f32x4{a[0].x, a[0].y, a[1].x, a[1].y}; mp[1] = f32x4{a[2].x, a[2].y, a[3].x, a[3].y};
    }
  }
  if (tid < 448) {
    LAS unsigned char *wp = S + LD_WSTG + srow * WSTG_ROW + scol * 16;
    *(LAS v4u *)(wp) = ws0;
    *(LAS v4u *)(wp + WSTG_M) = ws1;
    *(LAS v4u *)(wp + 2 * WSTG_M) = ws2;
  }
  asm volatile("" ::"v"(ws0), "v"(ws1), "v"(ws2), "v"(pAre), "v"(pAim), "v"(pBre), "v"(pBim), "v"(pCre), "v"(pCim));
  STAMP(1);
  BAR();
  if (xprod) {
    publish_xt(S, rs, lane, w, tau, tag);
  } else if (w < 6) {
    const int col = tid >> 4, part = tid & 15;
    LAS float *mp = (LAS float *)(S + LD_XROW) + part * 24 + col;
    float sm = part < 8 ? mp[160 * 24] : 0.f;
#pragma unroll
    for (int i = 0; i < 10; i++) sm += mp[i * 16 * 24];
    sm += dpp0<0x111, 0xF>(sm); sm += dpp0<0x112, 0xF>(sm); sm += dpp0<0x114, 0xF>(sm); sm += dpp0<0x118, 0xF>(sm);
    if (part == 15) st8(rs, WS_MODG + (24 * mi + col) * 8, v2u{__builtin_bit_cast(u32, rbf(sm)), tag});
  }
  PIN();
  STAMP(2);

  const float dt = rbf(__builtin_amdgcn_exp2f(bf2f(pLdt) * 1.44269504088896f));
  const float al = (dt * bf2f(pAre)) * 1.44269504088896f;
  const float rv = (dt * bf2f(pAim)) * 0.159154943091895f;
  const f2 Wn = cmul(f2{bf2f(pCre), -bf2f(pCim)}, f2{dt * bf2f(pBre), -dt * bf2f(pBim)});
  {
    const float mx = wave_max_nonneg(fmaxf(fabsf(Wn.x), fabsf(Wn.y)));
    if (lane == 0) *(LAS float *)(S + LD_WMAX + w * 4) = mx;
  }
  const u32 m0v = (u32)(LD_PQ + (w >> 2) * PQ_CH + (w & 3) * 256);
  const f2 p1 = zpow(al, rv), z8 = zpow(al * 8.f, rv * -8.f), Z1 = zpow(al * 128.f, rv * 128.f);
  const f2 p2 = cmul(p1, p1), p3 = cmul(p2, p1), p4 = cmul(p2, p2), p5 = cmul(p4, p1), p6 = cmul(p4, p2), p7 = cmul(p4, p3);
  pqw4<0>(m0v, f2{1.f, 0.f}, p1, p2, p3);
  pqw4<4>(m0v, p4, p5, p6, p7);
  pqw4<8>(m0v, Z1, cmul(Z1, p1), cmul(Z1, p2), cmul(Z1, p3));
  pqw4<12>(m0v, cmul(Z1, p4), cmul(Z1, p5), cmul(Z1, p6), cmul(Z1, p7));
  PIN();
  v4u wdfrag;
  {
    const int G = lane >> 4, e = (lane >> 2) & 3, pp = lane & 3, h = G >> 1;
    LAS unsigned char *a0 = S + LD_WSTG + (G & 1) * WSTG_M + (8 * h + e) * WSTG_ROW + (16 * w + 4 * pp) * 2;
    LAS unsigned char *a1 = a0 + (w < 6 ? 256 : 0);
    LAS unsigned char *a2 = S + LD_WSTG + 2 * WSTG_M + (4 * h + e) * WSTG_ROW + (32 * (w < 7 ? w : 6) + 16 * (G & 1) + 4 * pp) * 2;
    const v4u v0 = tr8(a0, a0 + 4 * WSTG_ROW), v1 = tr8(a1, a1 + 4 * WSTG_ROW);
    wdfrag = tr8(a2, a2 + 8 * WSTG_ROW);
    const int o1 = WS_W1F + ((14 * (m & 7) + w) * 28 + (m >> 3)) * 1024 + lane16;
    st16(rs, o1, v0);
    if (w < 6) st16(rs, o1 + 8 * 28 * 1024, v1);
    if (w == 7 && lane < 2 && m + 224 * lane < NCNT) st4(rs, WS_CNT + (m + 224 * lane) * 256, 0u);
  }
  PIN();
  STAMP(3);
  v4u xg[2], sg[2], mg[4], kt0;
  v2u kt1;
  int ebW;
  const int hch = (tid >> 2) & 1, l4 = 16 * (tid >> 3) + 4 * (tid & 3);
  const int xo = ((tid >> 3) * 224) * 128 + (tid & 7) * 16;
  const int xu = WS_XG + m * 128, mo = WS_MODG + 2 * m * 8;
#define LD_HANDOFF_A() do { \
    sg[0] = ld16s(rs, tid * 16, WS_STAT1); sg[1] = ld16s(rs, tid * 16, WS_STAT1 + 8192); \
    xg[0] = ld16s(rs, xo, xu); xg[1] = ld16s(rs, xo, xu + 64 * 224 * 128); \
    mg[0] = ld16s(rs, 0, mo + 0 * D * 8); mg[1] = ld16s(rs, 0, mo + 1 * D * 8); \
    mg[2] = ld16s(rs, 0, mo + 2 * D * 8); mg[3] = ld16s(rs, 0, mo + 4 * D * 8); } while (0)
  {
    const f2 Z2 = cmul(Z1, Z1), Z3 = cmul(Z2, Z1), Z4 = cmul(Z2, Z2);
    pqw4<16>(m0v, Z2, cmul(Z2, p2), cmul(Z2, p4), cmul(Z2, p6));
    pqw4<20>(m0v, Z3, cmul(Z3, p2), cmul(Z3, p4), cmul(Z3, p6));
    const f2 Z5 = cmul(Z4, Z1), Z6 = cmul(Z4, Z2), Z7 = cmul(Z4, Z3), Z8 = cmul(Z4, Z4);
    pqw4<24>(m0v, Z4, cmul(Z4, p4), Z5, cmul(Z5, p4));
    pqw4<28>(m0v, Z6, cmul(Z6, p4), Z7, cmul(Z7, p4));
    pqw4<32>(m0v, Z8, cmul(Z8, Z1), cmul(Z8, Z2), cmul(Z8, Z3));
    pqw4<36>(m0v, cmul(Z8, Z4), cmul(Z8, Z5), cmul(Z8, Z6), cmul(Z8, Z7));
    pqw1<40>(m0v, cmul(Z8, Z8));
    STAMP(20);
    kt0 = gld<v4u>(&g_ktab, (u32)t8 * 32u);
    kt1 = gld<v2u>(&g_ktab, (u32)t8 * 32u + 16u);
    LD_HANDOFF_A();
    PIN();
    asm volatile("s_waitcnt vmcnt(10) ; PUBWAIT" ::: "memory");
    BAR();
    if (tid == 0) st4(rs, WS_WRDY + m * 16, tag);
    STAMP(21);
    {
      const f32x4 mx4 = *(LAS f32x4 *)(S + LD_WMAX + (w & 4) * 4);
      const float mx = fmaxf(fmaxf(mx4[0], mx4[1]), fmaxf(mx4[2], mx4[3]));
      ebW = (int)((__builtin_bit_cast(u32, mx) >> 23) & 0xFF);
      ebW = ebW < 14 ? 14 : (ebW > 253 ? 253 : ebW);
    }
    const f2 z16 = cmul(z8, z8), z24 = cmul(z16, z8), z32 = cmul(z16, z16);
    f2 q0 = Wn * pow2b(267 - ebW), q1 = cmul(q0, z8), q2 = cmul(q0, z16), q3 = cmul(q0, z24);
    pqw4<41>(m0v, q0, q1, q2, q3);
    q0 = cmul(q0, z32); q1 = cmul(q1, z32); q2 = cmul(q2, z32); q3 = cmul(q3, z32);
    pqw4<45>(m0v, q0, q1, q2, q3);
    q0 = cmul(q0, z32); q1 = cmul(q1, z32); q2 = cmul(q2, z32); q3 = cmul(q3, z32);
    pqw4<49>(m0v, q0, q1, q2, q3);
    q0 = cmul(q0, z32); q1 = cmul(q1, z32); q2 = cmul(q2, z32); q3 = cmul(q3, z32);
    pqw4<53>(m0v, q0, q1, q2, q3);
  }
  STAMP(4);
  BAR();
  {
    const int tile = w & 3;
    if (tile < 3) {
      LAS unsigned char *pa = S + LD_PQ + cch * PQ_CH + (16 * tile + (lane & 15)) * PQ_ROW + (lane >> 4) * 16;
      LAS unsigned char *pb = S + LD_PQ + cch * PQ_CH + (41 + (lane & 15)) * PQ_ROW + (lane >> 4) * 16;
      f32x4 acc0 = {0.f, 0.f, 0.f, 0.f}, acc1 = acc0;
      v4u fa[16], fb[16];
#pragma unroll
      for (int ks = 0; ks < 16; ks++) { fa[ks] = *(LAS v4u *)(pa + ks * 64); fb[ks] = *(LAS v4u *)(pb + ks * 64); }
      PIN();
#pragma unroll
      for (int ks = 0; ks < 16; ks += 2) { acc0 = mfma16h(fa[ks], fb[ks], acc0); acc1 = mfma16h(fa[ks + 1], fb[ks + 1], acc1); }
      acc0 = (acc0 + acc1) * (1.0f / 256.0f);
      LAS u16 *kd = (LAS u16 *)(S + LD_KD + cch * KD_CH) + (16 * tile + 4 * (lane >> 4)) * 16 + (lane & 15);
      const u32 k01 = pkh(acc0[0], acc0[1]), k23 = pkh(acc0[2], acc0[3]);
      kd[0] = (u16)k01; kd[16] = (u16)(k01 >> 16); kd[32] = (u16)k23; kd[48] = (u16)(k23 >> 16);
    } else if (lane < 16) {
      ((LAS u16 *)(S + LD_KD + cch * KD_CH))[768 + lane] = 0;
    }
  }
#pragma unroll 1
  for (unsigned sp = 0; sp < SPIN_CAP; sp++) {
    const u32 d = ((xg[0].y ^ tag) | (xg[0].w ^ tag) | (xg[1].y ^ tag) | (xg[1].w ^ tag)) |
                  ((sg[0].y ^ tag) | (sg[0].w ^ tag) | (sg[1].y ^ tag) | (sg[1].w ^ tag)) |
                  ((mg[0].y ^ tag) | (mg[0].w ^ tag) | (mg[1].y ^ tag) | (mg[1].w ^ tag)) |
                  ((mg[2].y ^ tag) | (mg[2].w ^ tag) | (mg[3].y ^ tag) | (mg[3].w ^ tag));
    if (__all(d == 0)) break;
    __builtin_amdgcn_s_sleep(1);
    LD_HANDOFF_A();
  }
#undef LD_HANDOFF_A
  __builtin_amdgcn_fence(__ATOMIC_ACQUIRE, "wavefront");
  *(LAS f32x4 *)(S + LD_ST1 + tid * 16) = f32x4{bflo(sg[0].x), __builtin_amdgcn_rcpf(bfhi(sg[0].x)), bflo(sg[0].z), __builtin_amdgcn_rcpf(bfhi(sg[0].z))};
  *(LAS f32x4 *)(S + LD_ST1 + 8192 + tid * 16) = f32x4{bflo(sg[1].x), __builtin_amdgcn_rcpf(bfhi(sg[1].x)), bflo(sg[1].z), __builtin_amdgcn_rcpf(bfhi(sg[1].z))};
  STAMP(5);
  BAR();
  const int fo = WS_WRDY + t8 * 16;
  u32 wr = __builtin_amdgcn_raw_buffer_load_b32(rs, fo, 0, SC1);
  PIN();

  {
    LAS unsigned char *kd = S + LD_KD + cch * KD_CH;
    u32 v[11];
    const u32 ix[6] = {kt0.x, kt0.y, kt0.z, kt0.w, kt1.x, kt1.y};
#pragma unroll
    for (int i = 0; i < 11; i++) v[i] = *(LAS u16 *)(kd + ((i & 1) ? (ix[i >> 1] >> 16) : (ix[i >> 1] & 0xFFFFu)));
    LAS unsigned char *kc = S + LD_KC + cch * KC_CH + (16 + 8 * t8) * 2;
    u32 pr[10];
#pragma unroll
    for (int i = 0; i < 10; i++) pr[i] = v[i] | (v[i + 1] << 16);
#pragma unroll
    for (int e = 0; e < 4; e++) *(LAS v4u *)(kc + e * KC_CP) = v4u{pr[3 - e], pr[5 - e], pr[7 - e], pr[9 - e]};
    if (t8 < 8) *(LAS v4u *)(S + LD_KC + cch * KC_CH + (t8 >> 1) * KC_CP + (t8 & 1) * 16) = v4u{0, 0, 0, 0};
  }
  float A1, A2, c2;
  {
    const float sah = asf(hch ? mg[0].z : mg[0].x), c1h = rbf(1.0f + asf(hch ? mg[1].z : mg[1].x));
    int eh = (int)((__builtin_bit_cast(u32, 21.5f * fabsf(c1h) + fabsf(sah)) >> 23) & 0xFF);
    eh = eh < 14 ? 14 : (eh > 253 ? 253 : eh);
    const float sH = pow2b(267 - eh);
    const f2 c1s = {c1h * sH, c1h * sH}, sas = {sah * sH, sah * sH};
#pragma unroll
    for (int i = 0; i < 2; i++) {
      const u32 x01 = xg[i].x, x23 = xg[i].z;
      const f32x4 sa_ = *(LAS f32x4 *)(S + LD_ST1 + (l4 + 1024 * i) * 8), sb_ = *(LAS f32x4 *)(S + LD_ST1 + (l4 + 1024 * i) * 8 + 16);
      const f2 h01 = __builtin_elementwise_fma((f2{bflo(x01), bfhi(x01)} - f2{sa_[0], sa_[2]}) * f2{sa_[1], sa_[3]}, c1s, sas);
      const f2 h23 = __builtin_elementwise_fma((f2{bflo(x23), bfhi(x23)} - f2{sb_[0], sb_[2]}) * f2{sb_[1], sb_[3]}, c1s, sas);
      *(LAS v2u *)(S + LD_U + hch * U_CH + (256 + l4 + 1024 * i) * 2) = v2u{pkh(h01.x, h01.y), pkh(h23.x, h23.y)};
      *(LAS v2u *)(S + LD_XS + hch * 4096 + (l4 + 1024 * i) * 2) = v2u{x01, x23};
    }
    if (t8 < 32) *(LAS v4u *)(S + LD_U + cch * U_CH + 16 * t8) = v4u{0, 0, 0, 0};
    const float sac = asf(cch ? mg[0].z : mg[0].x), c1c = rbf(1.0f + asf(cch ? mg[1].z : mg[1].x));
    int ec = (int)((__builtin_bit_cast(u32, 21.5f * fabsf(c1c) + fabsf(sac)) >> 23) & 0xFF);
    ec = ec < 14 ? 14 : (ec > 253 ? 253 : ec);
    const float ga = asf(cch ? mg[2].z : mg[2].x);
    const float iH = pow2b(ec - 13);
    A1 = ga * iH * pow2b(ebW - 5);
    A2 = ga * bf2f(pDsk) * iH;
    c2 = rbf(1.0f + asf(cch ? mg[3].z : mg[3].x));
  }
#pragma unroll 1
  for (unsigned sp = 0; sp < SPIN_CAP; sp++) {
    if (__all(wr == tag)) break;
    __builtin_amdgcn_s_sleep(1);
    wr = __builtin_amdgcn_raw_buffer_load_b32(rs, fo, 0, SC1);
  }
  __builtin_amdgcn_fence(__ATOMIC_ACQUIRE, "wavefront");
  STAMP(6);
  BAR();
  if (b == 0 && tid == 64) st4(rs, WS_EPOCH, tag);
  if (w < 7) st16(rs, WS_WDF + ((7 * (m & 1) + w) * 112 + (m >> 1)) * 1024 + lane16, wdfrag);
  PIN();

  const int fc = 7 * (g & 1) + r % 7, tt = 4 * (g >> 1) + r / 7;
  const int phi = 8 * fc + w;
  const int dtile = r % 7, t2 = 4 * g + r / 7;
  v4u w1[28];
  const int w1o = WS_W1F + phi * 28 * 1024;
#define LDW1(s) do { w1[s] = ld16s(rs, lane16, w1o + (s) * 1024); PIN(); } while (0)
  v2u csg_;
  const int cso = WS_CSB + (phi * 64 + lane) * 8;

  {
    const int c = cch, pq = w & 3;
    const int a15 = lane & 15, q = lane >> 4, e = (lane >> 2) & 3, pp = lane & 3;
    LAS unsigned char *ka = S + LD_KC + c * KC_CH + e * KC_CP + (16 + 16 * (q >> 1) - 8 * (q & 1) + 4 * pp) * 2 + pq * 128 - 8;
    LAS unsigned char *ub = S + LD_U + c * U_CH + 512 + 32 * a15 - 32 * (q >> 1) + 16 * (q & 1) - pq * 128 - 64;
    const int l16 = 16 * a15 + 4 * q;
    const int xoff = ((q & 1) ? WS_X1S : WS_X1T) + ((a15 >> 2) * D + dch) * 128 + (16 * (a15 & 3) + 8 * (q >> 1)) * 2;
#define RD_A(i) tr8(ka + (i) * 512 + pi * 64 + 8, ka + (i) * 512 + pi * 64)
#define RD_B(i) (*(LAS v4u *)(ub + (i) * 512 + 64 - pi * 64))
#pragma unroll
    for (int hf = 0; hf < 2; hf++) {
      LAS unsigned char *ex = S + LD_EX + hf * 32768 + c * 16384 + lane16;
      f32x4 acc[4];
#pragma unroll
      for (int i = 0; i < 4; i++) acc[i] = f32x4{0.f, 0.f, 0.f, 0.f};
#pragma unroll
      for (int pi = 0; pi < 2; pi++) {
        v4u A[8], B[8];
        if (hf == 0) {
          A[0] = RD_A(0); B[0] = RD_B(0); B[1] = RD_B(1); B[2] = RD_B(2); B[3] = RD_B(3);
          A[1] = RD_A(1); A[2] = RD_A(2); A[3] = RD_A(3);
        } else {
          A[0] = RD_A(0); B[4] = RD_B(4); B[5] = RD_B(5); B[6] = RD_B(6); B[7] = RD_B(7);
          A[1] = RD_A(1); B[3] = RD_B(3); A[2] = RD_A(2); B[2] = RD_B(2); A[3] = RD_A(3); B[1] = RD_B(1);
          A[4] = RD_A(4); B[0] = RD_B(0); A[5] = RD_A(5); A[6] = RD_A(6); A[7] = RD_A(7);
        }
        PIN();
#pragma unroll
        for (int qq = 0; qq < 8; qq++) {
#pragma unroll
          for (int tl = 0; tl < 4; tl++)
            if (qq <= 4 * hf + tl) acc[tl] = mfma16h(A[qq], B[4 * hf + tl - qq], acc[tl]);
          PIN();
          if (hf == 0 && qq < 2) LDW1(2 * pi + qq);
          if (hf == 1 && qq < 7) LDW1(4 + 7 * pi + qq);
        }
      }
      STAMP(17 + 2 * hf);
#pragma unroll
      for (int tl = 0; tl < 4; tl++) *(LAS f32x4 *)(ex + (pq * 4 + tl) * 1024) = acc[tl];
      if (hf == 1) asm volatile("s_waitcnt vmcnt(14) ; PUBWAIT" ::: "memory");
      BAR();
      if (hf == 1) {
        if (tid < 8) __hip_atomic_fetch_add((gu32 *)(p.ws + WS_CNT) + (0 * 128 + tid * 16 + (m & 15)) * 64, 1u, RLX_AGENT);
        csg_ = ld8(rs, cso);
      }
      const int t = 4 * hf + pq;
      const int l0 = 256 * t + l16;
      const f32x4 y0 = *(LAS f32x4 *)(ex + (0 * 4 + pq) * 1024), y1 = *(LAS f32x4 *)(ex + (1 * 4 + pq) * 1024);
      const f32x4 y2 = *(LAS f32x4 *)(ex + (2 * 4 + pq) * 1024), y3 = *(LAS f32x4 *)(ex + (3 * 4 + pq) * 1024);
      const v2u hh = *(LAS v2u *)(S + LD_U + c * U_CH + (256 + l0) * 2);
      const v2u xx = *(LAS v2u *)(S + LD_XS + c * 4096 + l0 * 2);
      const f32x4 y = (y0 + y1) + (y2 + y3);
      const h2 ha = ash2(hh.x), hb = ash2(hh.y);
      const f2 a1 = {A1, A1}, a2 = {A2, A2}, cc = {c2, c2};
      const f2 o01 = __builtin_elementwise_fma(a2, f2{(float)ha.x, (float)ha.y},
                         __builtin_elementwise_fma(a1, f2{y[0], y[1]}, f2{bflo(xx.x), bfhi(xx.x)}));
      const f2 o23 = __builtin_elementwise_fma(a2, f2{(float)hb.x, (float)hb.y},
                         __builtin_elementwise_fma(a1, f2{y[2], y[3]}, f2{bflo(xx.y), bfhi(xx.y)}));
      const u32 X0 = pkbf(o01.x, o01.y), X1 = pkbf(o23.x, o23.y);
      const f2 s01 = f2{bflo(X0), bfhi(X0)} * cc, s23 = f2{bflo(X1), bfhi(X1)} * cc;
      const u32 Y0 = pkbf(s01.x, s01.y), Y1 = pkbf(s23.x, s23.y);
      const auto r0 = __builtin_amdgcn_permlane16_swap(X0, Y0, false, false);
      const auto r1 = __builtin_amdgcn_permlane16_swap(X1, Y1, false, false);
      st16(rs, xoff + 4 * t * D * 128, v4u{r0[0], r1[0], r0[1], r1[1]});
      PIN();
      STAMP(18 + 4 * hf);
    }
#pragma unroll
    for (int i = 18; i < 24; i++) LDW1(i);
    asm volatile("s_waitcnt vmcnt(6) ; PUBWAIT" ::: "memory");
    BAR();
    if (tid < 8) __hip_atomic_fetch_add((gu32 *)(p.ws + WS_CNT) + (1 * 128 + tid * 16 + (m & 15)) * 64, 1u, RLX_AGENT);
#undef RD_A
#undef RD_B
  }
  STAMP(7);

  if (w == 0) {
    poll_counters(p, 0 * 128 + g * 16, 16, 14u, lane);
  } else {
#pragma unroll
    for (int i = 24; i < 28; i++) LDW1(i);
  }
  BAR();
  STAMP(8);
  v4u xs[7];
  const int xso = tid * 16;
  const int xlo = (tid >> 3) * 128 + (((tid & 7) ^ (((tid >> 4) & 1) << 2)) * 16);
#pragma unroll
  for (int i = 0; i < 7; i++) xs[i] = ld16s(rs, xso, WS_X1S + tt * XB_SZ + i * 8192);
  PIN();
  if (w == 0) {
#pragma unroll
    for (int i = 24; i < 28; i++) LDW1(i);
  }
  *(LAS u32 *)(S + LD_CSL + (w * 64 + lane) * 4) = finish_granule(rs, cso, tag, csg_);
#define NWA 4
#define NWB 4
  constexpr int NWPF = NWA + NWB;
  v4u wd[28];
  const int wdo = WS_WDF + (2 * dtile * 112 + 14 * w) * 1024;
#define LDWD(i) (wd[i] = ld16s(rs, lane16, wdo + (((i) / 14) * 112 + (i) % 14) * 1024))
#pragma unroll
  for (int i = 0; i < 7; i++) {
    *(LAS v4u *)(S + LD_XB + xlo + i * 8192) = xs[i];
    if (i < NWA) LDWD(i);
    PIN();
  }
  BAR();
  STAMP(9);
  v2u sg2[2];
  u32 pk;
  {
    const int G = lane >> 4, e = (lane >> 2) & 3, pp = lane & 3, h = lane >> 5;
    const int sw = ((e >> 1) & 1) * 64;
    LAS unsigned char *bx0 = S + LD_XB + (8 * h + e) * 128 + ((0 + 32 * (G & 1) + 8 * pp) ^ sw);
    LAS unsigned char *bx1 = S + LD_XB + (8 * h + e) * 128 + ((64 + 32 * (G & 1) + 8 * pp) ^ sw);
    LAS unsigned char *cl = S + LD_CSL + w * 256 + 16 * h;
    const int ho = WS_HF + phi * 1024 + lane16;
    f32x16 acc[2][2];
    f2 ep1[2], ep2[2];
    u32 hp[2][4];
#define EPI_PART(hf_, j, gq) do { \
      const f32x4 csg = *(LAS f32x4 *)(cl + 32 * (gq)), csu = *(LAS f32x4 *)(cl + 32 * (gq) + 64); \
      const f32x4 bg = *(LAS f32x4 *)(cl + 32 * (gq) + 128), bu = *(LAS f32x4 *)(cl + 32 * (gq) + 192); \
      _Pragma("unroll") for (int i = 0; i < 4; i += 2) { \
        const f2 gg = __builtin_elementwise_fma(ep1[j], f2{acc[hf_][j][4 * (gq) + i], acc[hf_][j][4 * (gq) + i + 1]}, \
                          __builtin_elementwise_fma(ep2[j], f2{csg[i], csg[i + 1]}, f2{bg[i], bg[i + 1]})); \
        const f2 uu = __builtin_elementwise_fma(ep1[j], f2{acc[hf_][j][8 + 4 * (gq) + i], acc[hf_][j][8 + 4 * (gq) + i + 1]}, \
                          __builtin_elementwise_fma(ep2[j], f2{csu[i], csu[i + 1]}, f2{bu[i], bu[i + 1]})); \
        const f2 hv = gg * uu; \
        hp[j][2 * (gq) + i / 2] = pkbf(hv.x, hv.y); \
      } } while (0)
#define EPI_STORE(hf_, j) st16(rs, ho + (2 * (16 * (hf_) + tt) + (j)) * 112 * 1024, v4u{hp[j][0], hp[j][1], hp[j][2], hp[j][3]})
#define EPI_STATS(hf_) do { \
      const int sto_ = WS_STAT2 + (64 * (16 * (hf_) + tt) + (lane & 31)) * 8; \
      const u32 sv0 = finish_granule(rs, sto_, tag, sg2[0]), sv1 = finish_granule(rs, sto_ + 256, tag, sg2[1]); \
      const float r0_ = __builtin_amdgcn_rcpf(bfhi(sv0)), r1_ = __builtin_amdgcn_rcpf(bfhi(sv1)); \
      ep1[0] = f2{r0_, r0_}; ep1[1] = f2{r1_, r1_}; \
      ep2[0] = f2{-r0_ * bflo(sv0), -r0_ * bflo(sv0)}; ep2[1] = f2{-r1_ * bflo(sv1), -r1_ * bflo(sv1)}; } while (0)
    constexpr int NREM = 28 - NWPF;
#define WDSTEP(s) (((s) * NREM / 27 != ((s) + 1) * NREM / 27 && (s) < 27) ? NWPF + (s) * NREM / 27 : -1)
    constexpr int PUBSTEP = 10, PUBCNT = (PUBSTEP + 1) * NREM / 27;
#pragma unroll
    for (int hf = 0; hf < 2; hf++) {
#pragma unroll
      for (int j = 0; j < 2; j++)
#pragma unroll
        for (int i = 0; i < 16; i++) acc[hf][j][i] = 0.f;
      v4u fb0[3], fb1[3];
      LAS unsigned char *bh0 = bx0 + hf * XB_SZ, *bh1 = bx1 + hf * XB_SZ;
      asm volatile("" : "+v"(bh0), "+v"(bh1));
#define RD_X(s) do { const int o_ = (s) * 2048; fb0[(s) % 3] = tr8(bh0 + o_, bh0 + o_ + 512); fb1[(s) % 3] = tr8(bh1 + o_, bh1 + o_ + 512); } while (0)
      RD_X(0); RD_X(1);
#pragma unroll
      for (int s = 0; s < 28; s++) {
        if (s + 2 < 28) RD_X(s + 2);
        if (hf == 0) {
          if ((s & 1) == 0 && s / 2 < NWB) LDWD(NWA + s / 2);
          if (s == 3 && w == 0) pk = peek_counters(p, 1 * 128 + g * 16, 16, lane);
          if (s == 9) {
            if (w == 0) finish_counters(p, 1 * 128 + g * 16, 16, 14u, lane, pk);
            BAR();
          }
          if (s >= 9 && s < 16) xs[s - 9] = ld16s(rs, xso, WS_X1S + (16 + tt) * XB_SZ + (s - 9) * 8192);
          if (s >= 21) *(LAS v4u *)(S + LD_XB + XB_SZ + xlo + (s - 21) * 8192) = xs[s - 21];
        } else {
          if (WDSTEP(s) >= 0) LDWD(WDSTEP(s) < 0 ? 0 : WDSTEP(s));
          if (s == PUBSTEP) {
            asm volatile("s_waitcnt vmcnt(%0) ; PUBWAIT" ::"n"(PUBCNT) : "memory");
            BAR();
            if (tid == 0) __hip_atomic_fetch_add((gu32 *)(p.ws + WS_CNT) + (256 + tt) * 64, 1u, RLX_AGENT);
          }
        }
        if (s == 20) {
          const int sto = WS_STAT2 + (64 * (16 * hf + tt) + (lane & 31)) * 8;
          sg2[0] = ld8(rs, sto); sg2[1] = ld8(rs, sto + 256);
        }
        PIN();
        acc[hf][0] = mfma32b(w1[s], fb0[s % 3], acc[hf][0]);
        acc[hf][1] = mfma32b(w1[s], fb1[s % 3], acc[hf][1]);
        PIN();
        if (s == 3) STAMP(23 + 3 * hf);
        if (s == 27) STAMP(24 + 3 * hf);
      }
#undef RD_X
      if (hf == 0) {
        EPI_STATS(0);
        EPI_PART(0, 0, 0); EPI_PART(0, 0, 1); EPI_PART(0, 1, 0); EPI_PART(0, 1, 1);
        PIN();
        EPI_STORE(0, 0); EPI_STORE(0, 1);
        PIN();
        BAR();
        STAMP(10);
      }
    }
    EPI_STATS(1);
    STAMP(28);
    EPI_PART(1, 0, 0); EPI_PART(1, 0, 1); EPI_PART(1, 1, 0); EPI_PART(1, 1, 1);
    if (w == 0) pk = peek_counters(p, 256 + (t2 >> 1), 1, lane);
    PIN();
    EPI_STORE(1, 0); EPI_STORE(1, 1);
    PIN();
    STAMP(11);
#undef EPI_PART
#undef EPI_STORE
#undef EPI_STATS
  }

  if (w == 0) finish_counters(p, 256 + (t2 >> 1), 1, 14u, lane, pk);
  BAR();
  STAMP(12);
  v4u gbg[2], hb[14], res[2];
  const int gbo = WS_MODG + (5 * D + 64 * dtile + 4 * (2 * w + (lane >> 5))) * 8;
  const int reso = WS_X1T + ((t2 >> 1) * D + 64 * dtile + ((tid & 255) >> 2)) * 128 + (t2 & 1) * 64 + (tid & 3) * 16;
  const int hbo = WS_HF + (t2 * 112 + 14 * w) * 1024;
#pragma unroll
  for (int k = 0; k < 5; k++) hb[k] = ld16s(rs, lane16, hbo + k * 1024);
  PIN();
  asm volatile("s_waitcnt vmcnt(5) ; PUBWAIT" ::: "memory");
  BAR();
  if (tid == 0) __hip_atomic_fetch_add((gu32 *)(p.ws + WS_CNT) + (256 + 16 + tt) * 64, 1u, RLX_AGENT);
#pragma unroll
  for (int k = 5; k < 14; k++) hb[k] = ld16s(rs, lane16, hbo + k * 1024);
  res[0] = ld16s(rs, reso, 0); res[1] = ld16s(rs, reso, 16 * D * 128);
  gbg[0] = ld16(rs, gbo); gbg[1] = ld16(rs, gbo + 16);
  PIN();
#pragma unroll
  for (int hf = 0; hf < 2; hf++) {
    f32x16 acc[2];
#pragma unroll
    for (int j = 0; j < 2; j++)
#pragma unroll
      for (int i = 0; i < 16; i++) acc[j][i] = 0.f;
#pragma unroll
    for (int k = 0; k < 14; k++) {
      acc[0] = mfma32b(wd[k], hb[k], acc[0]);
      acc[1] = mfma32b(wd[14 + k], hb[k], acc[1]);
      if (hf == 0 && k == 9 && w == 0) { PIN(); pk = peek_counters(p, 256 + 16 + (t2 >> 1), 1, lane); PIN(); }
    }
    PIN();
    STAMP(29 + hf);
#define LDHL(k) do { if (hf == 0) { hb[k] = ld16s(rs, lane16, hbo + (32 * 112 + (k)) * 1024); PIN(); } } while (0)
    if (hf == 0) {
      if (w == 0) finish_counters(p, 256 + 16 + (t2 >> 1), 1, 14u, lane, pk);
      BAR();
    }
    {
      const int col = lane & 31, h = lane >> 5;
      LAS unsigned char *rd = S + LD_RED + w * RED_W + col * RED_ROW + h * 16;
#pragma unroll
      for (int i = 0; i < 8; i++) {
        const int dl = i >> 2, gq = i & 3;
        *(LAS f32x4 *)(rd + dl * 128 + gq * 32) = f32x4{acc[dl][4 * gq], acc[dl][4 * gq + 1], acc[dl][4 * gq + 2], acc[dl][4 * gq + 3]};
        PIN();
        LDHL(i);
      }
      if (tid < 256) *(LAS v4u *)(S + LD_RES + (tid >> 2) * 64 + (tid & 3) * 16) = res[hf];
    }
    if (hf == 0) {
#pragma unroll 1
      for (unsigned sp = 0; sp < SPIN_CAP; sp++) {
        if (__all(gbg[0].y == tag && gbg[0].w == tag && gbg[1].y == tag && gbg[1].w == tag)) break;
        gbg[0] = ld16(rs, gbo); gbg[1] = ld16(rs, gbo + 16);
      }
    }
    BAR();
    {
      const int tok = lane & 31, dq = 2 * w + (lane >> 5);
      f32x4 part[8];
#pragma unroll
      for (int ww = 0; ww < 8; ww++) part[ww] = *(LAS f32x4 *)(S + LD_RED + ww * RED_W + tok * RED_ROW + dq * 16);
      const v2u rr = trread(S + LD_RES + (4 * dq + ((lane >> 2) & 3)) * 64 + (16 * ((lane >> 4) & 1) + 4 * (lane & 3)) * 2);
      PIN();
      LDHL(8); LDHL(9);
      const f32x4 s0 = (part[0] + part[1]) + (part[2] + part[3]);
      PIN();
      LDHL(10); LDHL(11);
      const f32x4 sum = s0 + ((part[4] + part[5]) + (part[6] + part[7]));
      PIN();
      LDHL(12); LDHL(13);
      const u32 b01 = pkbf(sum[0], sum[1]), b23 = pkbf(sum[2], sum[3]);
      const u32 t01 = pkbf(asf(gbg[0].x) * bflo(b01), asf(gbg[0].z) * bfhi(b01));
      const u32 t23 = pkbf(asf(gbg[1].x) * bflo(b23), asf(gbg[1].z) * bfhi(b23));
      const float o0 = bflo(rr.x) + bflo(t01), o1 = bfhi(rr.x) + bfhi(t01);
      const float o2 = bflo(rr.y) + bflo(t23), o3 = bfhi(rr.y) + bfhi(t23);
      *(GAS v2u *)((GAS char *)p.out + (u32)((32 * (32 * hf + t2) + tok) * D + 64 * dtile + 4 * dq) * 2u) = v2u{pkbf(o0, o1), pkbf(o2, o3)};
    }
#undef LDHL
    STAMP(13 + 2 * hf);
    if (hf == 0) BAR();
  }
#undef LDW1
}

__device__ __forceinline__ void side_role(const Params &p, LAS unsigned char *S, const rsrc_t rs, const int tid,
                                          const int lane, const int w, const int b, const u32 epoch) {
  const int g = b & 7, r = b >> 3, sidx = 4 * g + r - 28;
  const int sc = sidx < 28 ? sidx : 27;
  const int lane16 = lane * 16;
  const int tau = 16 * g + (r >> 1);
  const u32 xo = (u32)(16 * tau + 2 * w) * (2u * D) + (u32)(lane < 56 ? lane : 55) * 16u;
  const v4u x0 = gld<v4u>(p.x, xo), x1 = gld<v4u>(p.x, xo + 2u * D);
  const u32 tag = __builtin_amdgcn_readfirstlane(epoch) + 1u;
  if (tid == 0) st4(rs, WS_WRDY + (224 + sidx) * 16, tag);
  if ((r & 1) == 0) {
    ln1_rows(S, x0, x1, w, lane);
    STAMP(1);
    BAR();
    publish_xt(S, rs, lane, w, tau, tag);
  }
  STAMP(3);
  {
    const int dd = tid < D ? tid : 0;
    const int o1 = WS_WRDY + (tid < 224 ? tid : 0) * 16, o2 = WS_MODG + (4 * D + dd) * 8, o3 = WS_MODG + (3 * D + dd) * 8;
    const v2u g2 = ld8(rs, o2), g3 = ld8(rs, o3);
    const u32 vcb = finish_granule(rs, o2, tag, g2), vsb = finish_granule(rs, o3, tag, g3);
    if (tid < D) {
      ((LAS u16 *)(S + LD_CV))[tid] = (u16)(pkbf(1.0f + asf(vcb), 0.f) & 0xFFFFu);
      ((LAS u16 *)(S + LD_CV))[D + tid] = (u16)(vsb >> 16);
    }
    if (w < 4) {
#pragma unroll 1
      for (unsigned sp = 0; sp < SPIN_CAP; sp++) {
        const u32 wr = __builtin_amdgcn_raw_buffer_load_b32(rs, o1, 0, SC1);
        if (__all(wr == tag)) break;
        __builtin_amdgcn_s_sleep(4);
      }
      __builtin_amdgcn_fence(__ATOMIC_ACQUIRE, "wavefront");
    }
  }
  BAR();
  STAMP(5);
  {
    const int ph = 4 * sc + (w >> 1), s0 = 14 * (w & 1);
    v4u wf[14];
#pragma unroll
    for (int k = 0; k < 14; k++) wf[k] = ld16s(rs, lane16, WS_W1F + (ph * 28 + s0 + k) * 1024);
    PIN();
    f32x16 acc;
#pragma unroll
    for (int i = 0; i < 16; i++) acc[i] = 0.f;
    LAS unsigned char *cvp = S + LD_CV + ((lane & 31) == 1 ? D * 2 : 0) + (lane >> 5) * 16;
#pragma unroll
    for (int k = 0; k < 14; k++) acc = mfma32b(wf[k], *(LAS v4u *)(cvp + (s0 + k) * 32), acc);
    LAS float *pr = (LAS float *)(S) + w * 64;
    if ((lane & 31) < 2) {
#pragma unroll
      for (int i = 0; i < 16; i++) pr[(lane & 31) * 32 + (i & 3) + 8 * (i >> 2) + 4 * (lane >> 5)] = acc[i];
    }
  }
  BAR();
  if (sidx < 28 && tid < 256) {
    const int pl = tid >> 6, e = tid & 63;
    const float v = ((LAS float *)S)[(2 * pl) * 64 + e] + ((LAS float *)S)[(2 * pl + 1) * 64 + e];
    st8(rs, WS_CSB + ((4 * sidx + pl) * 64 + e) * 8, v2u{__builtin_bit_cast(u32, v), tag});
  }
  STAMP(6);
#pragma unroll 1
  for (int hf = 0; hf < 2; hf++) {
    if (w == 0) poll_counters(p, hf * 128 + g * 16, 16, 14u, lane);
    BAR();
    const int tau2 = 32 * hf + sidx;
    const int tp = lane & 15, rgp = (lane >> 4) + 4 * w;
    u32 xv[14];
#pragma unroll
    for (int i = 0; i < 14; i++)
      xv[i] = __builtin_amdgcn_raw_buffer_load_b32(rs, rgp * 128 + tp * 4, WS_X1T + ((tau2 >> 1) * D + 32 * i) * 128 + (tau2 & 1) * 64, SC1);
    PIN();
    f2 sm = {0.f, 0.f}, sq = {0.f, 0.f};
#pragma unroll
    for (int i = 0; i < 14; i++) {
      const f2 a = {bflo(xv[i]), bfhi(xv[i])};
      sm += a; sq = __builtin_elementwise_fma(a, a, sq);
    }
    LAS f32x4 *pr = (LAS f32x4 *)(S + 4096);
    pr[rgp * 16 + tp] = f32x4{sm.x, sm.y, sq.x, sq.y};
    BAR();
    if (tid < 128) {
      const int tok = tid >> 2, part = tid & 3;
      LAS float *pf = (LAS float *)pr + (part * 8 * 16 + (tok >> 1)) * 4 + (tok & 1);
      float s = 0.f, q = 0.f;
#pragma unroll
      for (int i = 0; i < 8; i++) { s += pf[i * 64]; q += pf[i * 64 + 2]; }
      s += dpp0<0xB1, 0xF>(s); q += dpp0<0xB1, 0xF>(q);
      s += dpp0<0x4E, 0xF>(s); q += dpp0<0x4E, 0xF>(q);
      const float mean = s * (1.0f / D);
      const float var = rbf(fmaxf(q * (1.0f / D) - mean * mean, 0.f));
      const float sd = rbf(__builtin_amdgcn_sqrtf(rbf(var + 1e-5f)));
      if (part == 0) st8(rs, WS_STAT2 + (32 * tau2 + tok) * 8, v2u{pkbf(mean, sd), tag});
    }
    STAMP(8 + hf);
  }
}

__global__ void __launch_bounds__(NTHR, 2) k_main(Params p) {
  extern __shared__ __attribute__((aligned(16))) unsigned char smem[];
  LAS unsigned char *S = (LAS unsigned char *)smem;
  const int tid = threadIdx.x, lane = tid & 63, b = blockIdx.x;
  const int w = __builtin_amdgcn_readfirstlane(tid >> 6);
  const rsrc_t rs = __builtin_amdgcn_make_buffer_rsrc((void *)p.ws, 0, WS_END, 0x00020000);
  u32 pf;
  {
    unsigned long long pc;
    asm volatile("s_getpc_b64 %0" : "=s"(pc));
    pf = gld<u32>((const void *)(pc & ~127ull), (u32)(tid < PF_LINES ? tid : 0) * 128u);
  }
  const u32 epoch = __builtin_amdgcn_raw_buffer_load_b32(rs, WS_EPOCH, 0, SC1);
  STAMP(0);
  if ((b >> 3) < 28) main_role(p, S, rs, tid, lane, w, b, epoch);
  else side_role(p, S, rs, tid, lane, w, b, epoch);
  asm volatile("" ::"v"(pf));
}

}

extern "C" void kernel_launch(void *const *d_in, const int *in_sizes, int n_in, void *d_out, int out_size, void *d_ws,
                              size_t ws_size, hipStream_t stream) {
  using namespace mk;
  Params p;
  p.x = (const u16 *)d_in[0];
  p.Are = (const u16 *)d_in[1]; p.Aim = (const u16 *)d_in[2];
  p.Bre = (const u16 *)d_in[3]; p.Bim = (const u16 *)d_in[4];
  p.Cre = (const u16 *)d_in[5]; p.Cim = (const u16 *)d_in[6];
  p.logdt = (const u16 *)d_in[7]; p.Dsk = (const u16 *)d_in[8];
  p.wg = (const u16 *)d_in[10]; p.wu = (const u16 *)d_in[11]; p.wd = (const u16 *)d_in[12];
  p.cvec = (const u16 *)d_in[13]; p.wmod = (const u16 *)d_in[14];
  p.out = (u16 *)d_out;
  p.ws = (unsigned char *)d_ws;
  (void)hipFuncSetAttribute((const void *)k_main, hipFuncAttributeMaxDynamicSharedMemorySize, LDS_BYTES);
  k_main<<<NWG, NTHR, LDS_BYTES, stream>>>(p);
}
```
